```python
import torch

def _softmax(x, axis=-1):
    return torch.softmax(x, dim=axis)
def _gelu(x):
    return torch.nn.functional.gelu(x, approximate="tanh")
def _silu(x):
    return torch.nn.functional.silu(x)
def _sigmoid(x):
    return torch.sigmoid(x)
def _layernorm(x, axis=-1, eps=1e-5):
    m = x.mean(dim=axis, keepdim=True)
    v = x.var(dim=axis, keepdim=True, unbiased=False)
    return (x - m) / torch.sqrt(v + eps)
def _scatter_add(tgt, idx, src, axis=0):
    return tgt.clone().index_add_(axis, idx, src)
def _segment_sum(src, seg, n):
    out = torch.zeros((n,) + src.shape[1:], dtype=src.dtype)
    return out.index_add_(0, seg, src)
def _pad(x, pw):
    flat = tuple(v for pair in reversed(pw) for v in pair)
    return torch.nn.functional.pad(x, flat)

kernel_name = 'sequential_preLN__ssm_conv_h3__glu_gated_reglu__prompt'

def setup_inputs():

    _w = (lambda a: (lambda t: t.to(torch.bfloat16) if t.is_floating_point() else t)(torch.as_tensor(a)))
    def _A_setup():
        B, S, D = 1, 2048, 640
        ks = 4
        return {
            "q": _w((lambda *s: torch.randn(*s, dtype=torch.bfloat16) * 0.05)(B, S, D)),
            "k": _w((lambda *s: torch.randn(*s, dtype=torch.bfloat16) * 0.2)(B, S, D)),
            "v": _w((lambda *s: torch.randn(*s, dtype=torch.bfloat16) * 0.2)(B, S, D)),
            "shift_k": _w((lambda *s: torch.randn(*s, dtype=torch.bfloat16) * 0.2)(D, ks) * 0.5),
            "ssm_k": _w((lambda *s: torch.randn(*s, dtype=torch.bfloat16) * 0.4)(D, S) * (1.0 / S ** 0.5)),
            "ssm_D": _w((lambda *s: torch.randn(*s, dtype=torch.bfloat16) * 0.2)(D)),
        }
    _A_in = _A_setup()

    def _B_setup():
        B, S, D, F = 1, 2048, 640, 1728
        return {
            "x": _w((lambda *s: torch.randn(*s, dtype=torch.bfloat16) * 0.05)(B, S, D)),
            "W_gate": _w((lambda *s: torch.randn(*s, dtype=torch.bfloat16) * 0.2)(D, F) * (2.0 / D) ** 0.5),
            "W_up": _w((lambda *s: torch.randn(*s, dtype=torch.bfloat16) * 0.2)(D, F) * (2.0 / D) ** 0.5),
            "W_down": _w((lambda *s: torch.randn(*s, dtype=torch.bfloat16) * 0.2)(F, D) * (1.0 / F) ** 0.5),
        }
    _B_in = _B_setup()

    return {
        "A_q": _A_in["q"],
        "A_k": _A_in["k"],
        "A_v": _A_in["v"],
        "A_shift_k": _A_in["shift_k"],
        "A_ssm_k": _A_in["ssm_k"],
        "A_ssm_D": _A_in["ssm_D"],
        "B_x": _B_in["x"],
        "B_W_gate": _B_in["W_gate"],
        "B_W_up": _B_in["W_up"],
        "B_W_down": _B_in["W_down"],
    }

def reference(A_q, A_k, A_v, A_shift_k, A_ssm_k, A_ssm_D, B_x, B_W_gate, B_W_up, B_W_down):
    def _A_ref(q, k, v, shift_k, ssm_k, ssm_D):
        B, S, D = q.shape
        ks = shift_k.shape[-1]
        k_pad = _pad(k, ((0, 0), (ks - 1, 0), (0, 0)))
        k_shift = torch.zeros_like(k)
        for t in range(ks):
            k_shift = k_shift + k_pad[:, t:t + S, :] * shift_k[:, ks - 1 - t][None, None, :]
        kv = k_shift * v
        L = 2 * S
        kv_pad = _pad(kv, ((0, 0), (0, S), (0, 0)))
        kf_pad = _pad(ssm_k, ((0, 0), (0, S)))
        kv_f = (lambda x, axes=(-2,-1): torch.fft.rfft2(x.float(), dim=axes))(kv_pad[:, None, :, :], axes=(1, 2))
        kf_f = (lambda x, axes=(-2,-1): torch.fft.rfft2(x.float(), dim=axes))(kf_pad[:, None, :], axes=(1, 2))
        kf_f = (lambda x, *d: x.permute(*d))(kf_f, 1, 2, 0)
        y_f = kv_f * kf_f[None, :, :, :]
        y = (lambda x, s, axes=(-2,-1): torch.fft.irfft2(x, s=s, dim=axes).to(torch.bfloat16))(y_f, (1, L), axes=(1, 2))[:, 0, :S, :]
        y = y + kv * ssm_D[None, None, :]
        return q * y

    def _B_ref(x, W_gate, W_up, W_down):
        gate = torch.einsum("bsd,df->bsf", x, W_gate)
        up = torch.einsum("bsd,df->bsf", x, W_up)
        gate = gate * (lambda x, dt: x.to(dt))(gate > 0, gate.dtype)
        h = gate * up
        return torch.einsum("bsf,fd->bsd", h, W_down)

    x = A_q
    h = x + _A_ref(_layernorm(x, axis=-1), A_k, A_v, A_shift_k, A_ssm_k, A_ssm_D)
    h2 = h + _B_ref(_layernorm(h, axis=-1), B_W_gate, B_W_up, B_W_down)
    return h2
```

```cpp
#include <hip/hip_runtime.h>
#include <hip/hip_bf16.h>
#include <stdint.h>
typedef unsigned short u16;
constexpr int S=2048, D=640, F=1728;
__device__ __forceinline__ float b2f(u16 v){ return __uint_as_float(((unsigned)v)<<16); }
__device__ __forceinline__ u16 f2b(float f){ unsigned u=__float_as_uint(f); u += 0x7fffu + ((u>>16)&1u); return (u16)(u>>16); }
__device__ __forceinline__ float rb(float f){ return b2f(f2b(f)); }

__global__ void k_kv(const u16* k,const u16* v,const u16* sk,u16* kv){
  int i=blockIdx.x*blockDim.x+threadIdx.x; if(i>=S*D) return; int s=i/D,d=i%D;
  float acc=0.f;
  for(int t=0;t<4;++t){ int ss=s+t-3; float kk= ss>=0? b2f(k[ss*D+d]):0.f; float p=rb(kk*b2f(sk[d*4+3-t])); acc=rb(acc+p);}
  kv[i]=f2b(acc*b2f(v[i]));
}
__global__ void k_conv(const u16* kv,const u16* sk,const u16* sD,u16* y2){
  int i=blockIdx.x*blockDim.x+threadIdx.x; if(i>=S*D) return; int s=i/D,d=i%D;
  float acc=0.f;
  for(int j=0;j<=s;++j) acc+=b2f(sk[d*S+j])*b2f(kv[(s-j)*D+d]);
  float y=rb(acc); float t=rb(b2f(kv[i])*b2f(sD[d]));
  y2[i]=f2b(y+t);
}
__device__ void ln_stats(const float* v,int n,float& m,float& sd){
  float s=0; for(int i=0;i<n;++i) s+=v[i];
  for(int o=32;o;o>>=1) s+=__shfl_xor(s,o);
  float mean=s/D; float q=0; for(int i=0;i<n;++i){float t=v[i]-mean;q+=t*t;}
  for(int o=32;o;o>>=1) q+=__shfl_xor(q,o);
  float var=q/D; m=rb(mean); float vb=rb(var); float ve=rb(vb+1e-5f); sd=rb(sqrtf(ve));
}
__global__ void k_ln(const u16* x,const u16* y2,u16* h,u16* ln2){
  int r=blockIdx.x,l=threadIdx.x; float v[10];
  for(int i=0;i<10;++i) v[i]=b2f(x[r*D+l+64*i]);
  float m,sd; ln_stats(v,10,m,sd);
  for(int i=0;i<10;++i){ float q=rb(rb(v[i]-m)/sd); float g=rb(q*b2f(y2[r*D+l+64*i])); v[i]=rb(v[i]+g); h[r*D+l+64*i]=f2b(v[i]); }
  ln_stats(v,10,m,sd);
  for(int i=0;i<10;++i){ ln2[r*D+l+64*i]=f2b(rb(v[i]-m)/sd); }
}
__global__ void k_g1(const u16* a,const u16* wg,const u16* wu,u16* hm){
  int i=blockIdx.x*blockDim.x+threadIdx.x; if(i>=S*F) return; int s=i/F,f=i%F;
  float g=0,u=0; for(int k=0;k<D;++k){ float av=b2f(a[s*D+k]); g+=av*b2f(wg[k*F+f]); u+=av*b2f(wu[k*F+f]); }
  g=rb(g);u=rb(u); g= g>0.f? g: g*0.f; hm[i]=f2b(g*u);
}
__global__ void k_g2(const u16* hm,const u16* wd,const u16* h,u16* out){
  int i=blockIdx.x*blockDim.x+threadIdx.x; if(i>=S*D) return; int s=i/D,d=i%D;
  float o=0; for(int k=0;k<F;++k) o+=b2f(hm[s*F+k])*b2f(wd[k*D+d]);
  out[i]=f2b(b2f(h[i])+rb(o));
}
extern "C" void kernel_launch(void* const* d_in,const int* in_sizes,int n_in,void* d_out,int out_size,void* d_ws,size_t ws_size,hipStream_t stream){
  const u16 *q=(const u16*)d_in[0],*k=(const u16*)d_in[1],*v=(const u16*)d_in[2],*sk=(const u16*)d_in[3],*ssm=(const u16*)d_in[4],*sD=(const u16*)d_in[5];
  const u16 *wg=(const u16*)d_in[7],*wu=(const u16*)d_in[8],*wd=(const u16*)d_in[9];
  u16* ws=(u16*)d_ws; u16 *kv=ws,*y2=ws+S*D,*h=ws+2*S*D,*ln2=ws+3*S*D,*hm=ws+4*S*D;
  k_kv<<<S*D/256,256,0,stream>>>(k,v,sk,kv);
  k_conv<<<S*D/256,256,0,stream>>>(kv,ssm,sD,y2);
  k_ln<<<S,64,0,stream>>>(q,y2,h,ln2);
  k_g1<<<S*F/256,256,0,stream>>>(ln2,wg,wu,hm);
  k_g2<<<S*D/256,256,0,stream>>>(hm,wd,h,(u16*)d_out);
}
```

```cpp
#include <hip/hip_runtime.h>
#include <stdint.h>

typedef unsigned short u16;
typedef unsigned int u32;
typedef u32 u32x2 __attribute__((ext_vector_type(2)));
typedef u32 u32x4 __attribute__((ext_vector_type(4)));
typedef short s16x4 __attribute__((ext_vector_type(4)));
typedef short s16x8 __attribute__((ext_vector_type(8)));
typedef float f32x2 __attribute__((ext_vector_type(2)));
typedef float f32x4 __attribute__((ext_vector_type(4)));
typedef __bf16 bf16x2 __attribute__((ext_vector_type(2)));
#define LAS __attribute__((address_space(3)))
#define GAS __attribute__((address_space(1)))
#define DI __device__ __forceinline__

constexpr int S = 2048, D = 640, F = 1728;
constexpr float EPS = 1e-5f;

constexpr u32 WS_URT = 0;
constexpr u32 WS_XT  = WS_URT + D * S * 2;
constexpr u32 WS_HT  = WS_XT + D * S * 2;
constexpr u32 WS_ST1 = WS_HT + D * S * 2;
constexpr u32 WS_CS  = WS_ST1 + S * 4;
constexpr u32 WS_W1T = WS_CS + 16384;
constexpr u32 WS_W2T = WS_W1T + 16 * 20 * 14 * 1024;
constexpr u32 WS_HM  = WS_W2T + 8 * 54 * 5 * 1024;
constexpr u32 WS_END = WS_HM + 128 * 54 * 1024;

DI float blo(u32 u) { return __uint_as_float(u << 16); }
DI float bhi(u32 u) { return __uint_as_float(u & 0xffff0000u); }
DI u32 pk2(float a, float b) { bf16x2 r; r[0] = (__bf16)a; r[1] = (__bf16)b; return __builtin_bit_cast(u32, r); }
DI float rbf(float a) { return blo(pk2(a, a)); }
DI f32x4 mfma16(u32x4 a, u32x4 b, f32x4 c) {
  return __builtin_amdgcn_mfma_f32_16x16x32_bf16(__builtin_bit_cast(s16x8, a), __builtin_bit_cast(s16x8, b), c, 0, 0, 0);
}
DI float dot2(u32 a, u32 b, float c) { return __builtin_amdgcn_fdot2_f32_bf16(__builtin_bit_cast(bf16x2, a), __builtin_bit_cast(bf16x2, b), c, false); }
DI __amdgpu_buffer_rsrc_t mkrsrc(const void* p, u32 bytes) { return __builtin_amdgcn_make_buffer_rsrc((void*)p, 0, bytes, 0x00020000); }
constexpr int AUX_WT = 16;
DI void st128(__amdgpu_buffer_rsrc_t rs, u32 off, u32x4 v) { __builtin_amdgcn_raw_buffer_store_b128(v, rs, off, 0, AUX_WT); }
DI void st64(__amdgpu_buffer_rsrc_t rs, u32 off, u32x2 v) { __builtin_amdgcn_raw_buffer_store_b64(v, rs, off, 0, AUX_WT); }

__global__ __launch_bounds__(256) void k0_prep(const u16* __restrict__ x, const u16* __restrict__ k, const u16* __restrict__ v, const u16* __restrict__ shk, char* __restrict__ ws) {
  __shared__ __attribute__((aligned(16))) u32 sm[2 * 64 * 32];
  const int b = blockIdx.x, tid = threadIdx.x;
  if (b < 320) {
    const int tb = b / 10, cb = b % 10, c = tid & 7, rg = tid >> 3;
    const int t = 64 * tb + 2 * rg, ch0 = 64 * cb + 8 * c;
    u32x4 kr[5], vr[2], xr[2], w[4];
#pragma unroll
    for (int i = 0; i < 5; ++i) { const int row = t - 3 + i; kr[i] = row >= 0 ? *(const u32x4*)(k + row * D + ch0) : u32x4{0, 0, 0, 0}; }
#pragma unroll
    for (int i = 0; i < 2; ++i) { vr[i] = *(const u32x4*)(v + (t + i) * D + ch0); xr[i] = *(const u32x4*)(x + (t + i) * D + ch0); }
#pragma unroll
    for (int i = 0; i < 4; ++i) w[i] = *(const u32x4*)(shk + ch0 * 4 + 8 * i);
    u32* sU = sm; u32* sX = sm + 64 * 32;
    const int colU = 31 - rg, colX = rg;
    const int pU = 4 * ((colU >> 2) ^ c) + (colU & 3), pX = 4 * ((colX >> 2) ^ c) + (colX & 3);
#pragma unroll
    for (int j = 0; j < 8; ++j) {
      float kk[5];
#pragma unroll
      for (int i = 0; i < 5; ++i) kk[i] = (j & 1) ? bhi(kr[i][j >> 1]) : blo(kr[i][j >> 1]);
      const u32 wa = w[j >> 1][(j & 1) * 2], wb = w[j >> 1][(j & 1) * 2 + 1];
      const float w0 = blo(wa), w1 = bhi(wa), w2 = blo(wb), w3 = bhi(wb);
      const float s0 = kk[0] * w3 + kk[1] * w2 + kk[2] * w1 + kk[3] * w0;
      const float s1 = kk[1] * w3 + kk[2] * w2 + kk[3] * w1 + kk[4] * w0;
      const u32 sp = pk2(s0, s1);
      const float v0 = (j & 1) ? bhi(vr[0][j >> 1]) : blo(vr[0][j >> 1]);
      const float v1 = (j & 1) ? bhi(vr[1][j >> 1]) : blo(vr[1][j >> 1]);
      sU[(8 * c + j) * 32 + pU] = pk2(bhi(sp) * v1, blo(sp) * v0);
      sX[(8 * c + j) * 32 + pX] = (j & 1) ? __builtin_amdgcn_perm(xr[1][j >> 1], xr[0][j >> 1], 0x07060302u) : __builtin_amdgcn_perm(xr[1][j >> 1], xr[0][j >> 1], 0x05040100u);
    }
    __syncthreads();
    u16* uRT = (u16*)(ws + WS_URT); u16* xT = (u16*)(ws + WS_XT);
#pragma unroll
    for (int i = 0; i < 2; ++i) {
      const int piece = tid + 256 * i, ch = piece >> 3, g = piece & 7, p = ch * 32 + 4 * (g ^ (ch >> 3));
      *(u32x4*)(uRT + (64 * cb + ch) * S + (1984 - 64 * tb) + 8 * g) = *(const u32x4*)(sU + p);
      *(u32x4*)(xT + (64 * cb + ch) * S + 64 * tb + 8 * g) = *(const u32x4*)(sX + p);
    }
  } else {
    const int row = (b - 320) * 16 + (tid >> 4), s = tid & 15;
    u32x4 r[5];
#pragma unroll
    for (int i = 0; i < 5; ++i) r[i] = *(const u32x4*)(x + row * D + 8 * (s + 16 * i));
    float sum = 0.f;
#pragma unroll
    for (int i = 0; i < 5; ++i)
#pragma unroll
      for (int j = 0; j < 4; ++j) sum += blo(r[i][j]) + bhi(r[i][j]);
#pragma unroll
    for (int o = 1; o < 16; o <<= 1) sum += __shfl_xor(sum, o);
    const float mean = sum * (1.f / D);
    float q = 0.f;
#pragma unroll
    for (int i = 0; i < 5; ++i)
#pragma unroll
      for (int j = 0; j < 4; ++j) { const float a = blo(r[i][j]) - mean, c2 = bhi(r[i][j]) - mean; q += a * a + c2 * c2; }
#pragma unroll
    for (int o = 1; o < 16; o <<= 1) q += __shfl_xor(q, o);
    const float sd = sqrtf(rbf(rbf(q * (1.f / D)) + EPS));
    if (s == 0) ((u32*)(ws + WS_ST1))[row] = pk2(sd, mean);
  }
}

constexpr int NPRE1 = 54, NPRE2 = 68, NPRE = NPRE1 + NPRE2;
constexpr int KC_STRIDE = 4160;
constexpr int K1_RED = 24 * 1024;
constexpr int K1_U = K1_RED, K1_LDS = K1_U + 2304 * 2;

DI void tile8x8_store(const u32x4 (&r)[8], __amdgpu_buffer_rsrc_t rs, u32 off) {
#pragma unroll
  for (int jf = 0; jf < 8; ++jf) {
    const u32 sel = (jf & 1) ? 0x07060302u : 0x05040100u;
    u32x4 o;
#pragma unroll
    for (int p = 0; p < 4; ++p) o[p] = __builtin_amdgcn_perm(r[2 * p + 1][jf >> 1], r[2 * p][jf >> 1], sel);
    st128(rs, off + jf * 16, o);
  }
}

DI void pretile(int pb, const u16* wg, const u16* wu, const u16* wd, char* ws, char* smc) {
  const int tid = threadIdx.x;
  const __amdgpu_buffer_rsrc_t rs = mkrsrc(ws, WS_END);
  if (pb < NPRE1) {
    const int m = pb / 27, strip = pb % 27, fb = tid & 7, kg = tid >> 3;
    const u16* W = m ? wu : wg;
    const int col0 = 64 * strip + 8 * fb, ft = col0 >> 4;
    const int c = ft < 84 ? ft / 7 : 12 + (ft - 84) / 6, pair = ft < 84 ? ft % 7 : (ft - 84) % 6;
    float cs[8] = {0, 0, 0, 0, 0, 0, 0, 0};
#pragma unroll
    for (int r = 0; r < 3; ++r) {
      const int kb = kg + 32 * r;
      if (kb < 80) {
        u32x4 rr[8];
#pragma unroll
        for (int i = 0; i < 8; ++i) rr[i] = *(const u32x4*)(W + (8 * kb + i) * F + col0);
#pragma unroll
        for (int i = 0; i < 8; ++i)
#pragma unroll
          for (int j = 0; j < 4; ++j) { cs[2 * j] += blo(rr[i][j]); cs[2 * j + 1] += bhi(rr[i][j]); }
        const u32 off = WS_W1T + (((c * 20 + (kb >> 2)) * 14 + 2 * pair + m) * 64 + (col0 & 15) + 16 * (kb & 3)) * 16;
        tile8x8_store(rr, rs, off);
      }
    }
    float* red = (float*)smc;
#pragma unroll
    for (int j = 0; j < 8; ++j) red[kg * 65 + 8 * fb + j] = cs[j];
    __syncthreads();
    if (tid < 64) { float s = 0.f; for (int i = 0; i < 32; ++i) s += red[i * 65 + tid]; ((float*)(ws + WS_CS))[m * F + 64 * strip + tid] = s; }
  } else {
    const int g = (pb - NPRE1) * 256 + tid;
    if (g < 216 * 80) {
      const int db = g % 80, kq = g / 80, ks = kq >> 2, q = kq & 3, d0 = 8 * db;
      int lo, hi;
      if (ks < 36) { lo = 7 * (ks / 3) + 2 * (ks % 3); hi = lo + 1; }
      else if (ks < 48) { lo = 84 + 2 * (ks - 36); hi = lo + 1; }
      else { lo = 14 * (ks - 48) + 6; hi = lo + 7; }
      u32x4 rr[8];
#pragma unroll
      for (int i = 0; i < 8; ++i) rr[i] = *(const u32x4*)(wd + (16 * (i < 4 ? lo : hi) + 4 * q + (i & 3)) * D + d0);
      const u32 off = WS_W2T + ((((d0 / 80) * 54 + ks) * 5 + (d0 % 80) / 16) * 64 + (d0 & 15) + 16 * q) * 16;
      tile8x8_store(rr, rs, off);
    }
  }
}

__global__ __launch_bounds__(256) void k1_conv(const u16* __restrict__ ssmk, const u16* __restrict__ ssmD, const u16* __restrict__ wg, const u16* __restrict__ wu, const u16* __restrict__ wd, char* __restrict__ ws) {
  extern __shared__ __attribute__((aligned(16))) char sm[];
  const int b = blockIdx.x, tid = threadIdx.x;
  if (b < NPRE) { pretile(b, wg, wu, wd, ws, sm); return; }
  const int d = b - NPRE, lane = tid & 63, w = __builtin_amdgcn_readfirstlane(tid >> 6);
  const int l15 = lane & 15, q = lane >> 4;
  const u16* Kd = ssmk + d * S;
  u32x4 c0 = {0, 0, 0, 0}, c1 = {0, 0, 0, 0}, e0 = {0, 0, 0, 0}, e1 = {0, 0, 0, 0};
  if (tid >= 2) c0 = *(const u32x4*)(Kd + 8 * (tid - 2));
  if (tid >= 1) c1 = *(const u32x4*)(Kd + 8 * (tid - 1));
  if (tid < 2) { e0 = *(const u32x4*)(Kd + 8 * (254 + tid)); if (tid == 0) e1 = *(const u32x4*)(Kd + 8 * 255); }
  const u32x4 uu = *(const u32x4*)((const u16*)(ws + WS_URT) + d * S + 8 * tid);
  u32x2 xv[2]; u32x4 st[2];
#pragma unroll
  for (int i = 0; i < 2; ++i) {
    const int t = 256 * (2 * w + i) + 16 * l15 + 4 * q;
    xv[i] = *(const u32x2*)((const u16*)(ws + WS_XT) + d * S + t);
    st[i] = *(const u32x4*)((const u32*)(ws + WS_ST1) + t);
  }
  const float Dd = blo(ssmD[d]);
  auto build = [&](int m, const u32x4& a, const u32x4& bb) {
    const u32 ww[8] = {a[0], a[1], a[2], a[3], bb[0], bb[1], bb[2], bb[3]};
    u32x4 o0, o1, o2, o3;
#pragma unroll
    for (int i = 0; i < 4; ++i) {
      o0[i] = __builtin_amdgcn_alignbyte(ww[i + 1], ww[i], 2); o1[i] = ww[i + 1];
      o2[i] = __builtin_amdgcn_alignbyte(ww[i + 2], ww[i + 1], 2); o3[i] = ww[i + 2];
    }
    *(u32x4*)(sm + 0 * KC_STRIDE + 16 * m) = o0; *(u32x4*)(sm + 1 * KC_STRIDE + 16 * m) = o1;
    *(u32x4*)(sm + 2 * KC_STRIDE + 16 * m) = o2; *(u32x4*)(sm + 3 * KC_STRIDE + 16 * m) = o3;
  };
  build(tid, c0, c1);
  if (tid < 2) build(256 + tid, e0, e1);
  *(u32x4*)(sm + K1_U + 16 * tid) = uu;
  if (tid < 32) *(u32x4*)(sm + K1_U + 4096 + 16 * tid) = u32x4{0, 0, 0, 0};
  __syncthreads();
  f32x4 acc[8];
#pragma unroll
  for (int j = 0; j < 8; ++j) acc[j] = f32x4{0, 0, 0, 0};
#pragma unroll
  for (int rr = 0; rr < 2; ++rr) {
    const int rho = 2 * w + rr;
    const char* pf = sm + (l15 & 3) * KC_STRIDE + 2 * (l15 & 12) + 16 * q + 64 * rho;
    const char* pg = sm + K1_U + 480 - 32 * l15 + 64 * rho + 16 * q;
    u32x4 Ff[8], Gf[8];
#pragma unroll
    for (int i = 0; i < 8; ++i) {
      const u32x2 a = *(const u32x2*)(pf + 512 * i), bb = *(const u32x2*)(pf + 512 * i + 8);
      Ff[i] = u32x4{a[0], a[1], bb[0], bb[1]};
      Gf[i] = *(const u32x4*)(pg + 512 * (7 - i));
    }
#pragma unroll
    for (int i = 0; i < 8; ++i)
#pragma unroll
      for (int e = 0; e + i < 8; ++e) acc[i + e] = mfma16(Ff[i], Gf[e], acc[i + e]);
  }
  __syncthreads();
#pragma unroll
  for (int j = 0; j < 8; ++j) {
    const int o = j >> 1;
    if (o != w) *(f32x4*)(sm + (j * 3 + ((w - o - 1) & 3)) * 1024 + lane * 16) = acc[j];
  }
  __syncthreads();
  const __amdgpu_buffer_rsrc_t rs = mkrsrc(ws, WS_END);
#pragma unroll
  for (int i = 0; i < 2; ++i) {
    f32x4 y = (w == 0) ? acc[i] : (w == 1) ? acc[2 + i] : (w == 2) ? acc[4 + i] : acc[6 + i];
    const int j = 2 * w + i;
#pragma unroll
    for (int s = 0; s < 3; ++s) y += *(const f32x4*)(sm + (j * 3 + s) * 1024 + lane * 16);
    const int t = 256 * j + 16 * l15 + 4 * q;
    const u32x2 ur = *(const u32x2*)(sm + K1_U + 2 * (2044 - t));
    const float uv[4] = {bhi(ur[1]), blo(ur[1]), bhi(ur[0]), blo(ur[0])};
    const float xx[4] = {blo(xv[i][0]), bhi(xv[i][0]), blo(xv[i][1]), bhi(xv[i][1])};
    float h[4];
#pragma unroll
    for (int r = 0; r < 4; ++r) {
      const float mean = bhi(st[i][r]), rinv = 1.f / blo(st[i][r]);
      const float ln = rbf(rbf(xx[r] - mean) * rinv);
      const float y2 = rbf(rbf(y[r]) + rbf(uv[r] * Dd));
      h[r] = xx[r] + rbf(ln * y2);
    }
    st64(rs, WS_HT + (d * S + t) * 2, u32x2{pk2(h[0], h[1]), pk2(h[2], h[3])});
  }
}

template <int N> DI void waitv() { asm volatile("s_waitcnt vmcnt(%0)" ::"n"(N) : "memory"); }
DI void dma16(u32x4 srd, u32 lds, u32 voff, u32 soff) {
  asm volatile("s_mov_b32 m0, %0\n\ts_nop 0\n\tbuffer_load_dwordx4 %1, %2, %3 offen lds" ::"s"(lds), "v"(voff), "s"(srd), "s"(soff) : "memory");
}
DI u32x4 mksrd(const void* p, u32 bytes) { const unsigned long long a = (unsigned long long)p; return u32x4{(u32)a, (u32)(a >> 32) & 0xffffu, bytes, 0x00020000u}; }
DI u32 ldsaddr(const char* p) { return (u32)(unsigned long long)(LAS const char*)p; }
DI void bar() { __builtin_amdgcn_s_barrier(); }

constexpr int G1_NS = 7, G1_SLOT = 22 * 1024, G1_AUX = G1_NS * G1_SLOT, G1_LDS = G1_AUX + 2048, G1_NST = 20;
template <int NT, int CNT>
DI void g1_body(char* sm, char* ws, int rt, int c, int w, int lane) {
  constexpr int T0 = NT == 8 ? 0 : 8;
  const u32x4 srd = mksrd(ws, WS_END);
  const u32 lb = ldsaddr(sm);
  const int tw = w & 3, l15 = lane & 15, q = lane >> 4;
  u32 voff[3], sstep[3];
#pragma unroll
  for (int i = 0; i < CNT; ++i) {
    const int p = w + 8 * i;
    if (p < 14) { voff[i] = WS_W1T + (c * 20 * 14 + p) * 1024 + lane * 16; sstep[i] = 14 * 1024; }
    else { const int kk = 4 * (p - 14) + q, f = (kk & 3) | (((kk >> 3) & 1) << 2);
           voff[i] = WS_HT + kk * (S * 2) + rt * 256 + 16 * (l15 ^ (f << 1)); sstep[i] = 32 * S * 2; }
  }
  auto issue = [&](int ks, u32 slot) {
#pragma unroll
    for (int i = 0; i < CNT; ++i) dma16(srd, lb + slot + (w + 8 * i) * 1024, voff[i], ks * sstep[i]);
  };
  const int qp = l15 >> 2, p4 = lane & 3, msk = 2 * qp | ((q & 1) << 3);
  int hoff[2];
#pragma unroll
  for (int i = 0; i < 2; ++i) hoff[i] = 14336 + (8 * q + qp) * 256 + 16 * ((4 * tw + 2 * i + (p4 >> 1)) ^ msk) + 8 * (p4 & 1);
  const int woff = T0 * 1024 + lane * 16;
  struct Frags { u32x4 W[NT]; u32x4 H[2]; };
  auto rd = [&](Frags& f, u32 so) {
    const char* slot = sm + so;
#pragma unroll
    for (int i = 0; i < 2; ++i) {
      const s16x4 a = __builtin_amdgcn_ds_read_tr16_b64_v4i16((LAS s16x4*)(slot + hoff[i]));
      const s16x4 b = __builtin_amdgcn_ds_read_tr16_b64_v4i16((LAS s16x4*)(slot + hoff[i] + 1024));
      const u32x2 a2 = __builtin_bit_cast(u32x2, a), b2 = __builtin_bit_cast(u32x2, b);
      f.H[i] = u32x4{a2[0], a2[1], b2[0], b2[1]};
    }
#pragma unroll
    for (int m = 0; m < NT; ++m) f.W[m] = *(const u32x4*)(slot + woff + m * 1024);
  };
  f32x4 acc[2][NT];
#pragma unroll
  for (int i = 0; i < 2; ++i)
#pragma unroll
    for (int m = 0; m < NT; ++m) acc[i][m] = f32x4{0, 0, 0, 0};
  float stat[2] = {0.f, 0.f};
  auto mm = [&](const Frags& f) {
#pragma unroll
    for (int m = 0; m < NT; ++m)
#pragma unroll
      for (int i = 0; i < 2; ++i) acc[i][m] = mfma16(f.W[m], f.H[i], acc[i][m]);
#pragma unroll
    for (int i = 0; i < 2; ++i)
#pragma unroll
      for (int j = 0; j < 4; ++j) stat[i] = dot2(f.H[i][j], NT == 8 ? 0x3f803f80u : f.H[i][j], stat[i]);
  };
  const int f0 = c < 12 ? 112 * c : 1344 + 96 * (c - 12);
  if (w == 7) {
    const int l = lane < 56 ? lane : 55, m = l / 28, j = l % 28;
    int f = f0 + 4 * j; if (f > F - 4) f = F - 4;
    dma16(srd, lb + G1_AUX, WS_CS + (m * F + f) * 4, 0);
  }
#pragma unroll
  for (int s = 0; s < G1_NS - 1; ++s) issue(s, s * G1_SLOT);
  waitv<CNT * (G1_NS - 2)>();
  bar();
  Frags fa, fb;
  rd(fa, 0);
  u32 prev = (G1_NS - 1) * G1_SLOT, cur = 0;
  auto next = [&](u32 s) { return s == (G1_NS - 1) * G1_SLOT ? 0u : s + G1_SLOT; };
  auto step = [&](Frags& fc, Frags& fn, int n) {
    waitv<CNT * (G1_NS - 3)>();
    bar();
    issue(n + G1_NS - 1, prev);
    prev = cur; cur = next(cur);
    rd(fn, cur);
    mm(fc);
  };
  for (int n = 0; n < G1_NST - G1_NS + 1; n += 2) { step(fa, fb, n); step(fb, fa, n + 1); }
  static_assert(((G1_NST - G1_NS + 1) & 1) == 0 && G1_NS == 7, "tail below");
#define G1_TAIL(FC, FN, K) waitv<CNT * K>(); bar(); cur = next(cur); rd(FN, cur); mm(FC);
  G1_TAIL(fa, fb, 4) G1_TAIL(fb, fa, 3) G1_TAIL(fa, fb, 2) G1_TAIL(fb, fa, 1) G1_TAIL(fa, fb, 0)
  mm(fb);
  float* sx = (float*)(sm + G1_AUX + 1024);
#pragma unroll
  for (int i = 0; i < 2; ++i) {
    float s = stat[i]; s += __shfl_xor(s, 16); s += __shfl_xor(s, 32);
    if (lane < 16) sx[((NT == 8 ? 0 : 1) * 8 + 2 * tw + i) * 16 + lane] = s;
  }
  __syncthreads();
  const float* csl = (const float*)(sm + G1_AUX);
  const __amdgpu_buffer_rsrc_t rs = mkrsrc(ws, WS_END);
  const int ksb = 3 * c;
#pragma unroll
  for (int i = 0; i < 2; ++i) {
    const float s1 = sx[(2 * tw + i) * 16 + l15], s2 = sx[(8 + 2 * tw + i) * 16 + l15];
    const float mean = s1 * (1.f / D);
    const float var = fmaxf(s2 * (1.f / D) - mean * mean, 0.f);
    const float sd = rbf(sqrtf(rbf(rbf(var) + EPS)));
    const float mb = rbf(mean), rinv = 1.f / sd;
    const u32 ttile = 8 * rt + 2 * tw + i;
    u32 o[NT / 2][2];
#pragma unroll
    for (int pr = 0; pr < NT / 2; ++pr) {
      const int pg = T0 / 2 + pr;
      const f32x4 cg = *(const f32x4*)(csl + 16 * pg + 4 * q), cu = *(const f32x4*)(csl + 112 + 16 * pg + 4 * q);
      float hv[4];
#pragma unroll
      for (int r = 0; r < 4; ++r) {
        const float g = rbf((acc[i][2 * pr][r] - mb * cg[r]) * rinv), u = rbf((acc[i][2 * pr + 1][r] - mb * cu[r]) * rinv);
        hv[r] = fmaxf(g, 0.f) * u;
      }
      o[pr][0] = pk2(hv[0], hv[1]); o[pr][1] = pk2(hv[2], hv[3]);
    }
    if (NT == 8) {
      st128(rs, WS_HM + ((ttile * 54 + ksb) * 64 + lane) * 16, u32x4{o[0][0], o[0][1], o[1][0], o[1][1]});
      st128(rs, WS_HM + ((ttile * 54 + ksb + 1) * 64 + lane) * 16, u32x4{o[2 % (NT / 2)][0], o[2 % (NT / 2)][1], o[3 % (NT / 2)][0], o[3 % (NT / 2)][1]});
    } else {
      st128(rs, WS_HM + ((ttile * 54 + ksb + 2) * 64 + lane) * 16, u32x4{o[0][0], o[0][1], o[1][0], o[1][1]});
      if (c < 12) st64(rs, WS_HM + ((ttile * 54 + 48 + (c >> 1)) * 64 + lane) * 16 + 8 * (c & 1), u32x2{o[2][0], o[2][1]});
    }
  }
}
__global__ __launch_bounds__(512) void k2_gateup(char* __restrict__ ws) {
  extern __shared__ __attribute__((aligned(16))) char sm[];
  const int b = blockIdx.x, x = b & 7, i = b >> 3;
  const int rt = 8 * (x & 1) + (i & 7), c = 4 * (x >> 1) + (i >> 3);
  const int lane = threadIdx.x & 63, w = __builtin_amdgcn_readfirstlane(threadIdx.x >> 6);
  if (w < 4) g1_body<8, 3>(sm, ws, rt, c, w, lane);
  else if (w < 6) g1_body<6, 3>(sm, ws, rt, c, w, lane);
  else g1_body<6, 2>(sm, ws, rt, c, w, lane);
}

constexpr int G2_NS = 8, G2_SLOT = 18 * 1024, G2_AUX = G2_NS * G2_SLOT, G2_LDS = G2_AUX + 10 * 1024, G2_NST = 27;
template <int CNT>
DI void g2_body(char* sm, char* ws, u16* out, int rt, int dg, int w, int lane) {
  const int tt = w & 3, kh = w >> 2, l15 = lane & 15, q = lane >> 4;
  const u32x4 srd = mksrd(ws, WS_END);
  const u32 lb = ldsaddr(sm);
  u32 voff[3], sstep[3];
#pragma unroll
  for (int i = 0; i < CNT; ++i) {
    const int p = w + 8 * i;
    if (p < 10) { voff[i] = WS_W2T + (dg * 54 * 5 + p) * 1024 + lane * 16; sstep[i] = 10 * 1024; }
    else { const int pp = p - 10; voff[i] = WS_HM + ((4 * rt + (pp >> 1)) * 54 + (pp & 1)) * 1024 + lane * 16; sstep[i] = 2 * 1024; }
  }
  auto issue = [&](int st, u32 slot) {
#pragma unroll
    for (int i = 0; i < CNT; ++i) dma16(srd, lb + slot + (w + 8 * i) * 1024, voff[i], st * sstep[i]);
  };
  dma16(srd, lb + G2_AUX + w * 1024, WS_HT + (80 * dg + 8 * w + (lane >> 3)) * (S * 2) + rt * 128 + (lane & 7) * 16, 0);
  if (CNT == 3) dma16(srd, lb + G2_AUX + (w + 8) * 1024, WS_HT + (80 * dg + 8 * (w + 8) + (lane >> 3)) * (S * 2) + rt * 128 + (lane & 7) * 16, 0);
  struct Frags { u32x4 W[5]; u32x4 H; };
  const int woff = kh * 5 * 1024 + lane * 16, hoff = (10 + 2 * tt + kh) * 1024 + lane * 16;
  auto rd = [&](Frags& f, u32 so) {
    const char* slot = sm + so;
    f.H = *(const u32x4*)(slot + hoff);
#pragma unroll
    for (int m = 0; m < 5; ++m) f.W[m] = *(const u32x4*)(slot + woff + m * 1024);
  };
  f32x4 acc[5];
#pragma unroll
  for (int m = 0; m < 5; ++m) acc[m] = f32x4{0, 0, 0, 0};
  auto mm = [&](const Frags& f) {
#pragma unroll
    for (int m = 0; m < 5; ++m) acc[m] = mfma16(f.W[m], f.H, acc[m]);
  };
#pragma unroll
  for (int s = 0; s < G2_NS - 1; ++s) issue(s, s * G2_SLOT);
  waitv<CNT * (G2_NS - 2)>();
  bar();
  Frags fa, fb;
  rd(fa, 0);
  u32 prev = (G2_NS - 1) * G2_SLOT, cur = 0;
  auto next = [&](u32 s) { return s == (G2_NS - 1) * G2_SLOT ? 0u : s + G2_SLOT; };
  auto step = [&](Frags& fc, Frags& fn, int n) {
    waitv<CNT * (G2_NS - 3)>();
    bar();
    issue(n + G2_NS - 1, prev);
    prev = cur; cur = next(cur);
    rd(fn, cur);
    mm(fc);
  };
  for (int n = 0; n < G2_NST - G2_NS + 1; n += 2) { step(fa, fb, n); step(fb, fa, n + 1); }
  static_assert(((G2_NST - G2_NS + 1) & 1) == 0 && G2_NS == 8, "tail below");
#define G2_TAIL(FC, FN, K) waitv<CNT * K>(); bar(); cur = next(cur); rd(FN, cur); mm(FC);
  G2_TAIL(fa, fb, 5) G2_TAIL(fb, fa, 4) G2_TAIL(fa, fb, 3) G2_TAIL(fb, fa, 2) G2_TAIL(fa, fb, 1) G2_TAIL(fb, fa, 0)
  mm(fa);
  __syncthreads();
  if (kh == 1) {
#pragma unroll
    for (int m = 0; m < 5; ++m) *(f32x4*)(sm + (tt * 5 + m) * 1024 + lane * 16) = acc[m];
  }
  __syncthreads();
  if (kh == 0) {
    const u16* res = (const u16*)(sm + G2_AUX);
#pragma unroll
    for (int m = 0; m < 5; ++m) {
      const f32x4 o = acc[m] + *(const f32x4*)(sm + (tt * 5 + m) * 1024 + lane * 16);
      float r[4];
#pragma unroll
      for (int j = 0; j < 4; ++j) r[j] = __uint_as_float((u32)res[(16 * m + 4 * q + j) * 64 + 16 * tt + l15] << 16) + rbf(o[j]);
      *(u32x2*)(out + (64 * rt + 16 * tt + l15) * D + 80 * dg + 16 * m + 4 * q) = u32x2{pk2(r[0], r[1]), pk2(r[2], r[3])};
    }
  }
}
__global__ __launch_bounds__(512) void k3_down(char* __restrict__ ws, u16* __restrict__ out) {
  extern __shared__ __attribute__((aligned(16))) char sm[];
  const int b = blockIdx.x, x = b & 7, ii = b >> 3;
  const int rt = 8 * (x & 3) + (ii & 7), dg = 4 * (x >> 2) + (ii >> 3);
  const int lane = threadIdx.x & 63, w = __builtin_amdgcn_readfirstlane(threadIdx.x >> 6);
  if (w < 2) g2_body<3>(sm, ws, out, rt, dg, w, lane); else g2_body<2>(sm, ws, out, rt, dg, w, lane);
}

extern "C" void kernel_launch(void* const* d_in, const int* in_sizes, int n_in, void* d_out, int out_size, void* d_ws, size_t ws_size, hipStream_t stream) {
  const u16* q = (const u16*)d_in[0]; const u16* k = (const u16*)d_in[1]; const u16* v = (const u16*)d_in[2];
  const u16* shk = (const u16*)d_in[3]; const u16* ssmk = (const u16*)d_in[4]; const u16* ssmD = (const u16*)d_in[5];
  const u16* wg = (const u16*)d_in[7]; const u16* wu = (const u16*)d_in[8]; const u16* wd = (const u16*)d_in[9];
  char* ws = (char*)d_ws;
  (void)hipFuncSetAttribute((const void*)k2_gateup, hipFuncAttributeMaxDynamicSharedMemorySize, G1_LDS);
  (void)hipFuncSetAttribute((const void*)k3_down, hipFuncAttributeMaxDynamicSharedMemorySize, G2_LDS);
  k0_prep<<<448, 256, 0, stream>>>(q, k, v, shk, ws);
  k1_conv<<<NPRE + D, 256, K1_LDS, stream>>>(ssmk, ssmD, wg, wu, wd, ws);
  k2_gateup<<<256, 512, G1_LDS, stream>>>(ws);
  k3_down<<<256, 512, G2_LDS, stream>>>(ws, (u16*)d_out);
}
```

```cpp
#include <hip/hip_runtime.h>
#include <stdint.h>
#define NO_WARM 1

typedef unsigned short u16;
typedef unsigned int u32;
typedef u32 u32x2 __attribute__((ext_vector_type(2)));
typedef u32 u32x4 __attribute__((ext_vector_type(4)));
typedef short s16x4 __attribute__((ext_vector_type(4)));
typedef short s16x8 __attribute__((ext_vector_type(8)));
typedef float f32x2 __attribute__((ext_vector_type(2)));
typedef float f32x4 __attribute__((ext_vector_type(4)));
typedef __bf16 bf16x2 __attribute__((ext_vector_type(2)));
#define LAS __attribute__((address_space(3)))
#define DI __device__ __forceinline__

constexpr int S = 2048, D = 640, F = 1728;
constexpr float EPS = 1e-5f;

constexpr u32 WS_URT = 0;
constexpr u32 WS_XT  = WS_URT + D * S * 2;
constexpr u32 WS_HT  = WS_XT + D * S * 2;
constexpr u32 WS_ST1 = WS_HT + D * S * 2;
constexpr u32 WS_R2  = WS_ST1 + S * 4;
constexpr u32 WS_CNT = WS_R2 + S * 4;
constexpr u32 WS_W1T = WS_CNT + 16384;
constexpr u32 WS_W2T = WS_W1T + 16 * 20 * 14 * 1024;
constexpr u32 WS_HM  = WS_W2T + 8 * 54 * 5 * 1024;
constexpr u32 WS_END = WS_HM + 128 * 54 * 1024;

typedef __amdgpu_buffer_rsrc_t rsrc_t;
DI float blo(u32 u) { return __uint_as_float(u << 16); }
DI float bhi(u32 u) { return __uint_as_float(u & 0xffff0000u); }
DI u32 pk2(float a, float b) { bf16x2 r; r[0] = (__bf16)a; r[1] = (__bf16)b; return __builtin_bit_cast(u32, r); }
DI float rbf(float a) { return blo(pk2(a, a)); }
DI f32x4 mfma16(u32x4 a, u32x4 b, f32x4 c) {
  return __builtin_amdgcn_mfma_f32_16x16x32_bf16(__builtin_bit_cast(s16x8, a), __builtin_bit_cast(s16x8, b), c, 0, 0, 0);
}
DI rsrc_t mkrsrc(const void* p, u32 bytes) { return __builtin_amdgcn_make_buffer_rsrc((void*)p, 0, bytes, 0x00020000); }
constexpr int AUX_WT = 16, AUX_NT = 2;
DI u32x4 ld128nt(rsrc_t rs, u32 off) { return __builtin_amdgcn_raw_buffer_load_b128(rs, off, 0, AUX_NT); }
DI u32x4 ld128(rsrc_t rs, u32 off) { return __builtin_amdgcn_raw_buffer_load_b128(rs, off, 0, 0); }
DI u32x2 ld64(rsrc_t rs, u32 off) { return __builtin_amdgcn_raw_buffer_load_b64(rs, off, 0, 0); }
DI void st128(rsrc_t rs, u32 off, u32x4 v) { __builtin_amdgcn_raw_buffer_store_b128(v, rs, off, 0, AUX_WT); }
DI void st64(rsrc_t rs, u32 off, u32x2 v) { __builtin_amdgcn_raw_buffer_store_b64(v, rs, off, 0, AUX_WT); }
DI void pin() { __builtin_amdgcn_sched_barrier(0); }
DI u32x2 trd(const char* p) { return __builtin_bit_cast(u32x2, __builtin_amdgcn_ds_read_tr16_b64_v4i16((LAS s16x4*)p)); }
DI u32x4 tr_frag(const char* p0, const char* p1) { const u32x2 a = trd(p0), b = trd(p1); return u32x4{a[0], a[1], b[0], b[1]}; }

#define STAMP_DECL
#define ST(i) do {} while (0)
#define STAMP_OUT(K) do {} while (0)

__global__ __launch_bounds__(256) void k0_prep(const u16* __restrict__ x, const u16* __restrict__ k, const u16* __restrict__ v, const u16* __restrict__ shk, char* __restrict__ ws) {
  __shared__ __attribute__((aligned(16))) u32 sm[2 * 64 * 32];
  const int b = blockIdx.x, tid = threadIdx.x;
  STAMP_DECL ST(0);
  const rsrc_t rx = mkrsrc(x, S * D * 2);
  if (b == 447 && tid < 16) ((u32*)(ws + WS_CNT))[tid * 32] = 0u;
  if (b < 320) {
    const rsrc_t rk = mkrsrc(k, S * D * 2), rv = mkrsrc(v, S * D * 2), rw = mkrsrc(shk, D * 8);
    const int tb = b / 10, cb = b % 10, c = tid & 7, rg = tid >> 3;
    const int t = 64 * tb + 2 * rg, ch0 = 64 * cb + 8 * c;
    const u32 o = (u32)(t * D + ch0) * 2u;
    u32x4 kr[5], vr[2], xr[2], w[4];
#pragma unroll
    for (int i = 0; i < 5; ++i) kr[i] = ld128nt(rk, o + (u32)((i - 3) * D * 2));
#pragma unroll
    for (int i = 0; i < 2; ++i) { vr[i] = ld128nt(rv, o + i * D * 2); xr[i] = ld128nt(rx, o + i * D * 2); }
#pragma unroll
    for (int i = 0; i < 4; ++i) w[i] = ld128(rw, ch0 * 8 + 16 * i);
    pin();
    u32* sU = sm; u32* sX = sm + 64 * 32;
    const int colU = 31 - rg, colX = rg;
    const int pU = 4 * ((colU >> 2) ^ c) + (colU & 3), pX = 4 * ((colX >> 2) ^ c) + (colX & 3);
#pragma unroll
    for (int j = 0; j < 8; ++j) {
      float kk[5];
#pragma unroll
      for (int i = 0; i < 5; ++i) kk[i] = (j & 1) ? bhi(kr[i][j >> 1]) : blo(kr[i][j >> 1]);
      const u32 wa = w[j >> 1][(j & 1) * 2], wb = w[j >> 1][(j & 1) * 2 + 1];
      const float w0 = blo(wa), w1 = bhi(wa), w2 = blo(wb), w3 = bhi(wb);
      const float s0 = kk[0] * w3 + kk[1] * w2 + kk[2] * w1 + kk[3] * w0;
      const float s1 = kk[1] * w3 + kk[2] * w2 + kk[3] * w1 + kk[4] * w0;
      const u32 sp = pk2(s0, s1);
      const float v0 = (j & 1) ? bhi(vr[0][j >> 1]) : blo(vr[0][j >> 1]);
      const float v1 = (j & 1) ? bhi(vr[1][j >> 1]) : blo(vr[1][j >> 1]);
      sU[(8 * c + j) * 32 + pU] = pk2(bhi(sp) * v1, blo(sp) * v0);
      sX[(8 * c + j) * 32 + pX] = (j & 1) ? __builtin_amdgcn_perm(xr[1][j >> 1], xr[0][j >> 1], 0x07060302u) : __builtin_amdgcn_perm(xr[1][j >> 1], xr[0][j >> 1], 0x05040100u);
    }
    ST(1);
    __syncthreads();
#pragma unroll
    for (int i = 0; i < 2; ++i) {
      const int piece = tid + 256 * i, ch = piece >> 3, g = piece & 7, p = ch * 32 + 4 * (g ^ (ch >> 3));
      *(u32x4*)(ws + WS_URT + ((64 * cb + ch) * S + (1984 - 64 * tb) + 8 * g) * 2) = *(const u32x4*)(sU + p);
      *(u32x4*)(ws + WS_XT + ((64 * cb + ch) * S + 64 * tb + 8 * g) * 2) = *(const u32x4*)(sX + p);
    }
  } else {
    const int row = (b - 320) * 16 + (tid >> 4), s = tid & 15;
    u32x4 r[5];
#pragma unroll
    for (int i = 0; i < 5; ++i) r[i] = ld128nt(rx, (row * D + 8 * (s + 16 * i)) * 2);
    pin();
    float sum = 0.f;
#pragma unroll
    for (int i = 0; i < 5; ++i)
#pragma unroll
      for (int j = 0; j < 4; ++j) sum += blo(r[i][j]) + bhi(r[i][j]);
#pragma unroll
    for (int o = 1; o < 16; o <<= 1) sum += __shfl_xor(sum, o);
    const float mean = sum * (1.f / D);
    float q = 0.f;
#pragma unroll
    for (int i = 0; i < 5; ++i)
#pragma unroll
      for (int j = 0; j < 4; ++j) { const float a = blo(r[i][j]) - mean, c2 = bhi(r[i][j]) - mean; q += a * a + c2 * c2; }
#pragma unroll
    for (int o = 1; o < 16; o <<= 1) q += __shfl_xor(q, o);
    const float sd = __builtin_amdgcn_sqrtf(rbf(rbf(q * (1.f / D)) + EPS));
    if (s == 0) ((u32*)(ws + WS_ST1))[row] = pk2(sd, mean);
  }
  ST(2); STAMP_OUT(0);
}

constexpr int NW1 = 112;
constexpr int NTASK2 = 540, TPW = 2, NW2 = (NTASK2 + 4 * TPW - 1) / (4 * TPW);
constexpr int KC_STRIDE = 4160;
constexpr int K1_U = 32 * 1024, K1_LDS = K1_U + 2304 * 2;

DI void w1_strip(int r, const u16* wg, const u16* wu, char* ws, char* sm) {
  const int p = 8 * (r >> 4) + (r & 7), s = 2 * p + ((r >> 3) & 1);
  if (s >= 108) return;
  const int tid = threadIdx.x, lane = tid & 63, w = __builtin_amdgcn_readfirstlane(tid >> 6);
  const int m = s / 54, col0 = 32 * (s % 54), c4 = tid & 3, kg = tid >> 2;
  const rsrc_t rw = mkrsrc(m ? wu : wg, D * F * 2), rs = mkrsrc(ws, WS_END);
  u32x4 rr[10];
#pragma unroll
  for (int i = 0; i < 10; ++i) rr[i] = ld128nt(rw, ((kg + 64 * i) * F + col0 + 8 * c4) * 2);
  pin();
  const int g = lane >> 4, qp = (lane & 15) >> 2, p4 = lane & 3, c2 = w & 1;
  const u32x4 ones = {0x3f803f80u, 0x3f803f80u, 0x3f803f80u, 0x3f803f80u};
  u32x4 fr[10];
  f32x4 cs = {0, 0, 0, 0};
#pragma unroll
  for (int hf = 0; hf < 2; ++hf) {
    if (hf) __syncthreads();
#pragma unroll
    for (int i = 0; i < 5; ++i) *(u32x4*)(sm + (kg + 64 * i) * 80 + 16 * c4) = rr[5 * hf + i];
    __syncthreads();
#pragma unroll
    for (int i = 0; i < 5; ++i) {
      const char* im = sm + (32 * ((w >> 1) + 2 * i) + 8 * g + qp) * 80 + 32 * c2 + 8 * p4;
      fr[5 * hf + i] = tr_frag(im, im + 4 * 80);
      cs = mfma16(ones, fr[5 * hf + i], cs);
    }
  }
  float* red = (float*)(sm + 320 * 80);
  if (lane < 16) red[w * 16 + lane] = cs[0];
  __syncthreads();
  const float mu = (red[w * 16 + (lane & 15)] + red[(w ^ 2) * 16 + (lane & 15)]) * (1.f / D);
  const int ft = (col0 >> 4) + c2;
  const int c = ft < 84 ? ft / 7 : 12 + (ft - 84) / 6, pair = ft < 84 ? ft % 7 : (ft - 84) % 6;
#pragma unroll
  for (int i = 0; i < 10; ++i) {
    const int ks = 10 * (i / 5) + (w >> 1) + 2 * (i % 5);
    const u32x4 a = fr[i];
    st128(rs, WS_W1T + ((c * 20 + ks) * 14 + 2 * pair + m) * 1024 + lane * 16,
          u32x4{pk2(blo(a[0]) - mu, bhi(a[0]) - mu), pk2(blo(a[1]) - mu, bhi(a[1]) - mu), pk2(blo(a[2]) - mu, bhi(a[2]) - mu), pk2(blo(a[3]) - mu, bhi(a[3]) - mu)});
  }
}
DI void w2_tasks(int pb, const u16* wd, char* ws, char* sm) {
  const int tid = threadIdx.x, lane = tid & 63, w = __builtin_amdgcn_readfirstlane(tid >> 6);
  const rsrc_t rw = mkrsrc(wd, D * F * 2), rs = mkrsrc(ws, WS_END);
  char* im = sm + w * (32 * 144);
  u32x4 ld[TPW][4];
  int task[TPW];
#pragma unroll
  for (int u = 0; u < TPW; ++u) {
    int t = (pb * 4 + w) * TPW + u; if (t >= NTASK2) t = NTASK2 - 1;
    task[u] = t;
    const int ks = t / 10, dr = t % 10;
    int lo, hi;
    if (ks < 36) { lo = 7 * (ks / 3) + 2 * (ks % 3); hi = lo + 1; } else if (ks < 48) { lo = 84 + 2 * (ks - 36); hi = lo + 1; } else { lo = 14 * (ks - 48) + 6; hi = lo + 7; }
#pragma unroll
    for (int i = 0; i < 4; ++i) { const int r = (lane >> 3) + 8 * i; ld[u][i] = ld128nt(rw, ((16 * (r < 16 ? lo : hi) + (r & 15)) * D + 64 * dr + 8 * (lane & 7)) * 2); }
  }
  pin();
  const int g = lane >> 4, qp = (lane & 15) >> 2, p4 = lane & 3;
#pragma unroll
  for (int u = 0; u < TPW; ++u) {
#pragma unroll
    for (int i = 0; i < 4; ++i) *(u32x4*)(im + ((lane >> 3) + 8 * i) * 144 + 16 * (lane & 7)) = ld[u][i];
    const int ks = task[u] / 10;
#pragma unroll
    for (int c4 = 0; c4 < 4; ++c4) {
      const int dt = 4 * (task[u] % 10) + c4;
      const u32x4 fr = tr_frag(im + (4 * g + qp) * 144 + 32 * c4 + 8 * p4, im + (16 + 4 * g + qp) * 144 + 32 * c4 + 8 * p4);
      st128(rs, WS_W2T + (((dt / 5) * 54 + ks) * 5 + dt % 5) * 1024 + lane * 16, fr);
    }
  }
}

__global__ __launch_bounds__(256, 4) void k1_conv(const u16* __restrict__ ssmk, const u16* __restrict__ ssmD, const u16* __restrict__ wg, const u16* __restrict__ wu, const u16* __restrict__ wd, char* __restrict__ ws) {
  extern __shared__ __attribute__((aligned(16))) char sm[];
  const int b = blockIdx.x, tid = threadIdx.x;
  STAMP_DECL ST(0);
  if (b >= D + NW1) { w2_tasks(b - D - NW1, wd, ws, sm); ST(5); STAMP_OUT(1); return; }
  if (b >= D) { w1_strip(b - D, wg, wu, ws, sm); ST(5); STAMP_OUT(1); return; }
  const int d = b, lane = tid & 63, w = __builtin_amdgcn_readfirstlane(tid >> 6);
  const int l15 = lane & 15, q = lane >> 4;
  const rsrc_t rk = mkrsrc(ssmk + d * S, S * 2), rs = mkrsrc(ws, WS_END);
  const u32x4 c0 = ld128nt(rk, 16 * tid), c1 = ld128nt(rk, 16 * tid + 16);
  const u32x4 uu = ld128(rs, WS_URT + d * (S * 2) + 16 * tid);
  u32x2 xv[2]; u32x4 st[2];
#pragma unroll
  for (int i = 0; i < 2; ++i) {
    const int t = 256 * (2 * w + i) + 16 * l15 + 4 * q;
    xv[i] = ld64(rs, WS_XT + (d * S + t) * 2);
    st[i] = ld128(rs, WS_ST1 + t * 4);
  }
  const float Dd = blo(ssmD[d]);
  pin();
  ST(1);
  auto build = [&](int m, const u32x4& a, const u32x4& bb) {
    const u32 ww[8] = {a[0], a[1], a[2], a[3], bb[0], bb[1], bb[2], bb[3]};
    u32x4 o0, o1, o2, o3;
#pragma unroll
    for (int i = 0; i < 4; ++i) {
      o0[i] = __builtin_amdgcn_alignbyte(ww[i + 1], ww[i], 2); o1[i] = ww[i + 1];
      o2[i] = __builtin_amdgcn_alignbyte(ww[i + 2], ww[i + 1], 2); o3[i] = ww[i + 2];
    }
    *(u32x4*)(sm + 0 * KC_STRIDE + 16 * m) = o0; *(u32x4*)(sm + 1 * KC_STRIDE + 16 * m) = o1;
    *(u32x4*)(sm + 2 * KC_STRIDE + 16 * m) = o2; *(u32x4*)(sm + 3 * KC_STRIDE + 16 * m) = o3;
  };
  const u32x4 zero4 = {0, 0, 0, 0};
  build(tid + 2, c0, c1);
  if (tid < 2) build(1 - tid, zero4, tid == 0 ? c0 : zero4);
  *(u32x4*)(sm + K1_U + 16 * tid) = uu;
  if (tid < 32) *(u32x4*)(sm + K1_U + 4096 + 16 * tid) = zero4;
  __syncthreads();
  ST(2);
  f32x4 acc[8];
#pragma unroll
  for (int j = 0; j < 8; ++j) acc[j] = f32x4{0, 0, 0, 0};
#pragma unroll
  for (int rr = 0; rr < 2; ++rr) {
    const int rho = 2 * w + rr;
    const char* pf = sm + (l15 & 3) * KC_STRIDE + 2 * (l15 & 12) + 16 * q + 64 * rho;
    const char* pg = sm + K1_U + 480 - 32 * l15 + 64 * rho + 16 * q;
    u32x4 Ff[8], Gf[8];
#pragma unroll
    for (int i = 0; i < 8; ++i) {
      Gf[i] = *(const u32x4*)(pg + 512 * (7 - i));
      const u32x2 a = *(const volatile LAS u32x2*)(pf + 512 * i), bb = *(const volatile LAS u32x2*)(pf + 512 * i + 8);
      Ff[i] = u32x4{a[0], a[1], bb[0], bb[1]};
    }
#pragma unroll
    for (int i = 0; i < 8; ++i)
#pragma unroll
      for (int e = 0; e + i < 8; ++e) acc[i + e] = mfma16(Ff[i], Gf[e], acc[i + e]);
  }
  ST(3);
  __syncthreads();
#pragma unroll
  for (int j = 0; j < 8; ++j) *(f32x4*)(sm + (j * 4 + w) * 1024 + lane * 16) = acc[j];
  __syncthreads();
  ST(4);
#pragma unroll
  for (int i = 0; i < 2; ++i) {
    const int j = 2 * w + i;
    f32x4 y = *(const f32x4*)(sm + (j * 4) * 1024 + lane * 16);
#pragma unroll
    for (int s = 1; s < 4; ++s) y += *(const f32x4*)(sm + (j * 4 + s) * 1024 + lane * 16);
    const int t = 256 * j + 16 * l15 + 4 * q;
    const u32x2 ur = *(const u32x2*)(sm + K1_U + 2 * (2044 - t));
    const float uv[4] = {bhi(ur[1]), blo(ur[1]), bhi(ur[0]), blo(ur[0])};
    const float xx[4] = {blo(xv[i][0]), bhi(xv[i][0]), blo(xv[i][1]), bhi(xv[i][1])};
    float h[4];
#pragma unroll
    for (int r = 0; r < 4; ++r) {
      const float ln = (xx[r] - bhi(st[i][r])) * __builtin_amdgcn_rcpf(blo(st[i][r]));
      h[r] = fmaf(ln, fmaf(uv[r], Dd, y[r]), xx[r]);
    }
    *(u32x2*)(ws + WS_HT + (d * S + t) * 2) = u32x2{pk2(h[0], h[1]), pk2(h[2], h[3])};
  }
  ST(5); STAMP_OUT(1);
}

template <int N> DI void waitv() { asm volatile("s_waitcnt vmcnt(%0)" ::"n"(N) : "memory"); }
DI void dma16(u32x4 srd, u32 lds, u32 voff, u32 soff) {
  asm volatile("s_mov_b32 m0, %0\n\ts_nop 0\n\tbuffer_load_dwordx4 %1, %2, %3 offen lds" ::"s"(lds), "v"(voff), "s"(srd), "s"(soff) : "memory");
}
DI u32x4 mksrd(const void* p, u32 bytes) { const unsigned long long a = (unsigned long long)p; return u32x4{(u32)a, (u32)(a >> 32) & 0xffffu, bytes, 0x00020000u}; }
DI u32 ldsaddr(const char* p) { return (u32)(unsigned long long)(LAS const char*)p; }
DI void bar() { __builtin_amdgcn_s_barrier(); }

template <int CNT, int NS, int NST, int SLOT>
struct Loader {
  u32x4 srd; u32 lb; u32 voff[CNT], sstep[CNT], ldst[CNT]; int rot;
  DI void issue(int st, u32 slot) {
    int sr = st + rot; if (sr >= NST) sr -= NST;
#pragma unroll
    for (int i = 0; i < CNT; ++i) dma16(srd, lb + slot + ldst[i], voff[i], sr * sstep[i]);
  }
  DI void prologue() {
    static_assert(NS >= 4 && NS <= 8, "");
#pragma unroll
    for (int s = 0; s < NS - 2; ++s) issue(s, s * SLOT);
  }
  DI void run(int nwarm = 0, u32 wlds = 0, u32 wvoff = 0, u32 wstep = 0) {
    waitv<CNT * (NS - 3)>();
    bar();
    issue(NS - 2, (NS - 2) * SLOT);
    u32 slot = (NS - 1) * SLOT;
    for (int n = 0; n + NS - 1 < NST; ++n) {
      waitv<CNT * (NS - 3)>();
      bar();
      issue(n + NS - 1, slot);
      if (n < nwarm) dma16(srd, wlds, wvoff, n * wstep);
      slot = slot == (NS - 1) * SLOT ? 0u : slot + SLOT;
    }
#pragma unroll
    for (int k = NS - 3; k >= 0; --k) {
      if (k == 5) waitv<CNT * 5>(); else if (k == 4) waitv<CNT * 4>(); else if (k == 3) waitv<CNT * 3>(); else if (k == 2) waitv<CNT * 2>(); else if (k == 1) waitv<CNT>(); else waitv<0>();
      bar();
    }
  }
};

constexpr int G1_NS = 6, G1_SLOT = 22 * 1024, G1_NST = 20;
constexpr int G2_NS = 8, G2_SLOT = 10 * 1024, G2_RING = 2 * G1_SLOT, G2_NST = 27;
#define G2HW 9
constexpr int G2_HW = G2HW;
constexpr int G_AUX = G1_NS * G1_SLOT, G_LDS = G_AUX + 11 * 1024;
static_assert(G2_RING + G2_NS * G2_SLOT <= G_AUX, "phase B ring lies in the slots that phase A has left");
typedef __attribute__((address_space(1))) u32 gu32;

template <int CNTA, int CNTB>
DI void mlp_loader(char* sm, char* ws, int rt, int c, int rt3, int dg, int rotA, int rotB, int wi, int lw, int lane) {
  const u32x4 srd = mksrd(ws, WS_END);
  const u32 lb = ldsaddr(sm);
  {
#pragma unroll
    for (int i = 0; i < 3; ++i) {
      const int p = lw + 4 * i;
      if (p < 10) dma16(srd, lb + G_AUX + p * 1024, WS_HT + (80 * dg + 8 * p + (lane >> 3)) * (S * 2) + rt3 * 128 + (lane & 7) * 16, 0);
    }
    Loader<CNTA, G1_NS, G1_NST, G1_SLOT> L;
    L.srd = srd; L.lb = lb; L.rot = rotA;
    const int l15 = lane & 15, q = lane >> 4;
#pragma unroll
    for (int i = 0; i < CNTA; ++i) {
      const int p = lw + 4 * i;
      L.ldst[i] = p * 1024;
      if (p < 14) { L.voff[i] = WS_W1T + (c * 20 * 14 + p) * 1024 + lane * 16; L.sstep[i] = 14 * 1024; }
      else { const int kk = 4 * (p - 14) + q, f = (kk & 3) | (((kk >> 3) & 1) << 2);
             L.voff[i] = WS_HT + kk * (S * 2) + rt * 256 + 16 * (l15 ^ (f << 1)); L.sstep[i] = 32 * S * 2; }
    }
    L.prologue();
    L.run();
  }
  Loader<CNTB, G2_NS, G2_NST, G2_SLOT> L;
  L.srd = srd; L.lb = lb + G2_RING; L.rot = rotB;
#pragma unroll
  for (int i = 0; i < CNTB; ++i) { const int p = lw + 4 * i; L.ldst[i] = p * 1024; L.voff[i] = WS_W2T + (dg * 54 * 5 + p) * 1024 + lane * 16; L.sstep[i] = 10 * 1024; }
  L.prologue();
  bar();
  L.run();
  __syncthreads();
  __syncthreads();
}

template <int NT, int T0, bool STAT>
DI void g1_body(char* sm, char* ws, int rt, int c, int w, int lane) {
  const int tw = w & 3, l15 = lane & 15, q = lane >> 4;
  const int qp = l15 >> 2, p4 = lane & 3, msk = 2 * qp | ((q & 1) << 3);
  int hoff[2];
#pragma unroll
  for (int i = 0; i < 2; ++i) hoff[i] = 14336 + (8 * q + qp) * 256 + 16 * ((4 * tw + 2 * i + (p4 >> 1)) ^ msk) + 8 * (p4 & 1);
  const int woff = T0 * 1024 + lane * 16;
  u32x4 W[NT];
  u32x2 h0a, h0b, h1a, h1b;
  f32x4 acc[2][NT], accS[2], accG[2];
#pragma unroll
  for (int i = 0; i < 2; ++i) {
    accS[i] = f32x4{0, 0, 0, 0}; accG[i] = f32x4{0, 0, 0, 0};
#pragma unroll
    for (int m = 0; m < NT; ++m) acc[i][m] = f32x4{0, 0, 0, 0};
  }
  const u32x4 ones = {0x3f803f80u, 0x3f803f80u, 0x3f803f80u, 0x3f803f80u};
  auto half0 = [&](const char* slot) {
    const u32x4 h0 = {h0a[0], h0a[1], h0b[0], h0b[1]};
#pragma unroll
    for (int m = 0; m < NT; ++m) {
      acc[0][m] = mfma16(W[m], h0, acc[0][m]);
      if (m == 0) h1a = trd(slot + hoff[1]);
      if (m == 1) h1b = trd(slot + hoff[1] + 1024);
      pin();
    }
    if (STAT) { accS[0] = mfma16(ones, h0, accS[0]); accG[0] = mfma16(h0, h0, accG[0]); pin(); }
  };
  auto half1 = [&](const char* nslot, bool doread) {
    const u32x4 h1 = {h1a[0], h1a[1], h1b[0], h1b[1]};
#pragma unroll
    for (int m = 0; m < NT; ++m) {
      acc[1][m] = mfma16(W[m], h1, acc[1][m]);
      if (doread) {
        W[m] = *(const u32x4*)(nslot + woff + m * 1024);
        if (m == 0) h0a = trd(nslot + hoff[0]);
        if (m == 1) h0b = trd(nslot + hoff[0] + 1024);
      }
      pin();
    }
    if (STAT) { accS[1] = mfma16(ones, h1, accS[1]); accG[1] = mfma16(h1, h1, accG[1]); pin(); }
  };
  bar();
#pragma unroll
  for (int m = 0; m < NT; ++m) {
    W[m] = *(const u32x4*)(sm + woff + m * 1024);
    if (m == 0) h0a = trd(sm + hoff[0]);
    if (m == 1) h0b = trd(sm + hoff[0] + 1024);
  }
  u32 cur = 0;
  half0(sm);
  for (int n = 0; n < G1_NST - 1; ++n) {
    bar();
    cur = cur == (G1_NS - 1) * G1_SLOT ? 0u : cur + G1_SLOT;
    half1(sm + cur, true);
    half0(sm + cur);
  }
  half1(sm, false);
  const rsrc_t rs = mkrsrc(ws, WS_END);
  const int ksb = 3 * c;
#pragma unroll
  for (int i = 0; i < 2; ++i) {
    const u32 ttile = 8 * rt + 2 * tw + i;
    u32 o[NT / 2][2];
#pragma unroll
    for (int pr = 0; pr < NT / 2; ++pr) {
      const f32x4 g = acc[i][2 * pr], u = acc[i][2 * pr + 1];
      o[pr][0] = pk2(fmaxf(g[0], 0.f) * u[0], fmaxf(g[1], 0.f) * u[1]); o[pr][1] = pk2(fmaxf(g[2], 0.f) * u[2], fmaxf(g[3], 0.f) * u[3]);
    }
    if (NT == 8) {
      st128(rs, WS_HM + ((ttile * 54 + ksb) * 64 + lane) * 16, u32x4{o[0][0], o[0][1], o[1][0], o[1][1]});
      st128(rs, WS_HM + ((ttile * 54 + ksb + 1) * 64 + lane) * 16, u32x4{o[2 % (NT / 2)][0], o[2 % (NT / 2)][1], o[3 % (NT / 2)][0], o[3 % (NT / 2)][1]});
    } else {
      st128(rs, WS_HM + ((ttile * 54 + ksb + 2) * 64 + lane) * 16, u32x4{o[0][0], o[0][1], o[1][0], o[1][1]});
      if (NT == 6) st64(rs, WS_HM + ((ttile * 54 + 48 + (c >> 1)) * 64 + lane) * 16 + 8 * (c & 1), u32x2{o[2 % (NT / 2)][0], o[2 % (NT / 2)][1]});
    }
    if (STAT) {
      if (tw == c - 12 && q == (l15 >> 2)) {
        const int r = l15 & 3;
        const float s2 = r == 0 ? accG[i][0] : r == 1 ? accG[i][1] : r == 2 ? accG[i][2] : accG[i][3];
        const float mean = accS[i][0] * (1.f / D), var = fmaxf(s2 * (1.f / D) - mean * mean, 0.f);
        const float rinv = __builtin_amdgcn_rcpf(rbf(__builtin_amdgcn_sqrtf(rbf(rbf(var) + EPS))));
        __builtin_amdgcn_raw_buffer_store_b32(__float_as_uint(rinv * rinv), rs, WS_R2 + (16 * ttile + l15) * 4, 0, AUX_WT);
      }
    }
  }
}

DI void g2_body(char* sm, char* ws, u16* out, int rt3, int dg, int rot, int w, int lane) {
  const int tt = w & 3, kh = w >> 2, l15 = lane & 15, q = lane >> 4;
  const rsrc_t rs = mkrsrc(ws, WS_END);
  const u32 hbase = WS_HM + ((4 * rt3 + tt) * 54 + kh) * 1024 + lane * 16;
  u32x4 H[G2_HW];
  auto hload = [&](int n) { int sr = n + rot; if (sr >= G2_NST) sr -= G2_NST;
    return __builtin_amdgcn_raw_buffer_load_b128(rs, hbase, sr * 2048, AUX_WT);
  };
#pragma unroll
  for (int j = 0; j < G2_HW; ++j) H[j] = hload(j);
  const float r2 = __uint_as_float(__builtin_amdgcn_raw_buffer_load_b32(rs, WS_R2 + (64 * rt3 + 16 * tt + l15) * 4, 0, AUX_WT));
  pin();
  const char* wb = sm + G2_RING + kh * 5 * 1024 + lane * 16;
  u32x4 Wa[5], Wb[5];
  f32x4 acc[5];
#pragma unroll
  for (int m = 0; m < 5; ++m) { acc[m] = f32x4{0, 0, 0, 0}; Wa[m] = *(const u32x4*)(wb + m * 1024); }
  auto step = [&](u32x4 (&Wc)[5], u32x4 (&Wn)[5], int n) {
    if (n + 1 < G2_NST) bar();
#pragma unroll
    for (int m = 0; m < 5; ++m) {
      if (n + 1 < G2_NST) Wn[m] = *(const u32x4*)(wb + ((n + 1) % G2_NS) * G2_SLOT + m * 1024);
      acc[m] = mfma16(Wc[m], H[n % G2_HW], acc[m]);
      pin();
    }
    if (n + G2_HW < G2_NST) { H[n % G2_HW] = hload(n + G2_HW); pin(); }
  };
#pragma unroll
  for (int n = 0; n < G2_NST; ++n) { if (n & 1) step(Wb, Wa, n); else step(Wa, Wb, n); }
  __syncthreads();
  if (kh == 1) {
#pragma unroll
    for (int m = 0; m < 5; ++m) *(f32x4*)(sm + (tt * 5 + m) * 1024 + lane * 16) = acc[m];
  }
  __syncthreads();
  if (kh == 0) {
    const u16* res = (const u16*)(sm + G_AUX);
#pragma unroll
    for (int m = 0; m < 5; ++m) {
      const f32x4 o = acc[m] + *(const f32x4*)(sm + (tt * 5 + m) * 1024 + lane * 16);
      float r[4];
#pragma unroll
      for (int j = 0; j < 4; ++j) r[j] = __uint_as_float((u32)res[(16 * m + 4 * q + j) * 64 + 16 * tt + l15] << 16) + rbf(o[j] * r2);
      *(u32x2*)(out + (64 * rt3 + 16 * tt + l15) * D + 80 * dg + 16 * m + 4 * q) = u32x2{pk2(r[0], r[1]), pk2(r[2], r[3])};
    }
  }
}

__global__ __launch_bounds__(768) void k2_mlp(char* __restrict__ ws, u16* __restrict__ out) {
  extern __shared__ __attribute__((aligned(16))) char sm[];
  const int b = blockIdx.x, x = b & 7, i = b >> 3, cq = x >> 1;
  const int rt = 8 * (x & 1) + (i & 7), c = 4 * cq + (i >> 3);
  const int rt3 = 2 * rt + (cq >> 1), dg = 4 * (cq & 1) + (c & 3);
  const int lane = threadIdx.x & 63, w = __builtin_amdgcn_readfirstlane(threadIdx.x >> 6);
#define ROTA 0
#define ROTB 0
  const int rotA = ROTA ? (5 * (c & 3) + (5 * (i & 7)) / 2) % G1_NST : 0, rotB = ROTB ? (7 * (dg & 3) + (27 * (i & 7)) / 8) % G2_NST : 0;
  if (w >= 8) {
    if (w < 10) mlp_loader<6, 3>(sm, ws, rt, c, rt3, dg, rotA, rotB, i, w - 8, lane); else mlp_loader<5, 2>(sm, ws, rt, c, rt3, dg, rotA, rotB, i, w - 8, lane);
    return;
  }
  STAMP_DECL ST(0);
  if (w < 4) g1_body<8, 0, false>(sm, ws, rt, c, w, lane);
  else if (c < 12) g1_body<6, 8, false>(sm, ws, rt, c, w, lane);
  else g1_body<4, 8, true>(sm, ws, rt, c, w, lane);
  ST(1);
  asm volatile("s_waitcnt vmcnt(0)" ::: "memory");
  bar();
  ST(2);
  if (threadIdx.x == 0) {
    gu32* cnt = (gu32*)(ws + WS_CNT) + rt * 32;
    __hip_atomic_fetch_add(cnt, 1u, __ATOMIC_RELAXED, __HIP_MEMORY_SCOPE_AGENT);
    for (u32 spins = 0; __hip_atomic_load(cnt, __ATOMIC_RELAXED, __HIP_MEMORY_SCOPE_AGENT) < 16u && spins < (1u << 20); ++spins) __builtin_amdgcn_s_sleep(1);
  }
  __builtin_amdgcn_fence(__ATOMIC_ACQUIRE, "wavefront");
  bar();
  ST(3);
  g2_body(sm, ws, out, rt3, dg, rotB, w, lane);
  ST(5); STAMP_OUT(2);
}

extern "C" void kernel_launch(void* const* d_in, const int* in_sizes, int n_in, void* d_out, int out_size, void* d_ws, size_t ws_size, hipStream_t stream) {
  const u16* q = (const u16*)d_in[0]; const u16* k = (const u16*)d_in[1]; const u16* v = (const u16*)d_in[2];
  const u16* shk = (const u16*)d_in[3]; const u16* ssmk = (const u16*)d_in[4]; const u16* ssmD = (const u16*)d_in[5];
  const u16* wg = (const u16*)d_in[7]; const u16* wu = (const u16*)d_in[8]; const u16* wd = (const u16*)d_in[9];
  char* ws = (char*)d_ws;
  (void)hipFuncSetAttribute((const void*)k2_mlp, hipFuncAttributeMaxDynamicSharedMemorySize, G_LDS);
  k0_prep<<<448, 256, 0, stream>>>(q, k, v, shk, ws);
  k1_conv<<<D + NW1 + NW2, 256, K1_LDS, stream>>>(ssmk, ssmD, wg, wu, wd, ws);
  k2_mlp<<<256, 768, G_LDS, stream>>>(ws, (u16*)d_out);
}
```

```cpp
#include <hip/hip_runtime.h>
#include <stdint.h>
#define NO_WARM 1

typedef unsigned short u16;
typedef unsigned int u32;
typedef u32 u32x2 __attribute__((ext_vector_type(2)));
typedef u32 u32x4 __attribute__((ext_vector_type(4)));
typedef short s16x4 __attribute__((ext_vector_type(4)));
typedef short s16x8 __attribute__((ext_vector_type(8)));
typedef float f32x2 __attribute__((ext_vector_type(2)));
typedef float f32x4 __attribute__((ext_vector_type(4)));
typedef __bf16 bf16x2 __attribute__((ext_vector_type(2)));
#define LAS __attribute__((address_space(3)))
#define DI __device__ __forceinline__

constexpr int S = 2048, D = 640, F = 1728;
constexpr float EPS = 1e-5f;

constexpr u32 WS_URT = 0;
constexpr u32 WS_XT  = WS_URT + D * S * 2;
constexpr u32 WS_HT  = WS_XT + D * S * 2;
constexpr u32 WS_ST1 = WS_HT + D * S * 2;
constexpr u32 WS_R2  = WS_ST1 + S * 4;
constexpr u32 WS_CNT = WS_R2 + S * 4;
constexpr u32 WS_W1T = WS_CNT + 16384;
constexpr u32 WS_W2T = WS_W1T + 16 * 20 * 14 * 1024;
constexpr u32 WS_HM  = WS_W2T + 8 * 54 * 5 * 1024;
constexpr u32 WS_END = WS_HM + 128 * 54 * 1024;

typedef __amdgpu_buffer_rsrc_t rsrc_t;
DI float blo(u32 u) { return __uint_as_float(u << 16); }
DI float bhi(u32 u) { return __uint_as_float(u & 0xffff0000u); }
DI u32 pk2(float a, float b) { bf16x2 r; r[0] = (__bf16)a; r[1] = (__bf16)b; return __builtin_bit_cast(u32, r); }
DI float rbf(float a) { return blo(pk2(a, a)); }
DI f32x4 mfma16(u32x4 a, u32x4 b, f32x4 c) {
  return __builtin_amdgcn_mfma_f32_16x16x32_bf16(__builtin_bit_cast(s16x8, a), __builtin_bit_cast(s16x8, b), c, 0, 0, 0);
}
DI rsrc_t mkrsrc(const void* p, u32 bytes) { return __builtin_amdgcn_make_buffer_rsrc((void*)p, 0, bytes, 0x00020000); }
constexpr int AUX_WT = 16, AUX_NT = 2;
DI u32x4 ld128nt(rsrc_t rs, u32 off) { return __builtin_amdgcn_raw_buffer_load_b128(rs, off, 0, AUX_NT); }
DI u32x4 ld128(rsrc_t rs, u32 off) { return __builtin_amdgcn_raw_buffer_load_b128(rs, off, 0, 0); }
DI u32x2 ld64(rsrc_t rs, u32 off) { return __builtin_amdgcn_raw_buffer_load_b64(rs, off, 0, 0); }
template <int AUX = AUX_WT> DI void st128(rsrc_t rs, u32 off, u32x4 v) { __builtin_amdgcn_raw_buffer_store_b128(v, rs, off, 0, AUX); }
template <int AUX = AUX_WT> DI void st64(rsrc_t rs, u32 off, u32x2 v) { __builtin_amdgcn_raw_buffer_store_b64(v, rs, off, 0, AUX); }
constexpr int P_HT = 0, P_K0 = 0, P_W = 16;
DI void pin() { __builtin_amdgcn_sched_barrier(0); }
DI u32x2 trd(const char* p) { return __builtin_bit_cast(u32x2, __builtin_amdgcn_ds_read_tr16_b64_v4i16((LAS s16x4*)p)); }
DI u32x4 tr_frag(const char* p0, const char* p1) { const u32x2 a = trd(p0), b = trd(p1); return u32x4{a[0], a[1], b[0], b[1]}; }

#define STAMP_DECL
#define STAMP_PARAM
#define STAMP_PASS
#define ST(i) do {} while (0)
#define STAMP_OUT(K) do {} while (0)

__global__ __launch_bounds__(256) void k0_prep(const u16* __restrict__ x, const u16* __restrict__ k, const u16* __restrict__ v, const u16* __restrict__ shk, char* __restrict__ ws) {
  __shared__ __attribute__((aligned(16))) u32 sm[2 * 64 * 32];
  const int b = blockIdx.x, tid = threadIdx.x;
  STAMP_DECL ST(0);
  const rsrc_t rx = mkrsrc(x, S * D * 2);
  if (b == 447 && tid < 16) ((u32*)(ws + WS_CNT))[tid * 32] = 0u;
  if (b < 320) {
    const rsrc_t rk = mkrsrc(k, S * D * 2), rv = mkrsrc(v, S * D * 2), rw = mkrsrc(shk, D * 8);
    const int tb = (b & 7) + 8 * ((b >> 3) / 10), cb = (b >> 3) % 10, c = tid & 7, rg = tid >> 3;
    const int t = 64 * tb + 2 * rg, ch0 = 64 * cb + 8 * c;
    const u32 o = (u32)(t * D + ch0) * 2u;
    u32x4 kr[5], vr[2], xr[2], w[4];
#pragma unroll
    for (int i = 0; i < 5; ++i) kr[i] = ld128nt(rk, o + (u32)((i - 3) * D * 2));
#pragma unroll
    for (int i = 0; i < 2; ++i) { vr[i] = ld128nt(rv, o + i * D * 2); xr[i] = ld128nt(rx, o + i * D * 2); }
#pragma unroll
    for (int i = 0; i < 4; ++i) w[i] = ld128(rw, ch0 * 8 + 16 * i);
    pin();
    u32* sU = sm; u32* sX = sm + 64 * 32;
    const int colU = 31 - rg, colX = rg;
    const int pU = 4 * ((colU >> 2) ^ c) + (colU & 3), pX = 4 * ((colX >> 2) ^ c) + (colX & 3);
#pragma unroll
    for (int j = 0; j < 8; ++j) {
      float kk[5];
#pragma unroll
      for (int i = 0; i < 5; ++i) kk[i] = (j & 1) ? bhi(kr[i][j >> 1]) : blo(kr[i][j >> 1]);
      const u32 wa = w[j >> 1][(j & 1) * 2], wb = w[j >> 1][(j & 1) * 2 + 1];
      const float w0 = blo(wa), w1 = bhi(wa), w2 = blo(wb), w3 = bhi(wb);
      const float s0 = kk[0] * w3 + kk[1] * w2 + kk[2] * w1 + kk[3] * w0;
      const float s1 = kk[1] * w3 + kk[2] * w2 + kk[3] * w1 + kk[4] * w0;
      const u32 sp = pk2(s0, s1);
      const float v0 = (j & 1) ? bhi(vr[0][j >> 1]) : blo(vr[0][j >> 1]);
      const float v1 = (j & 1) ? bhi(vr[1][j >> 1]) : blo(vr[1][j >> 1]);
      sU[(8 * c + j) * 32 + pU] = pk2(bhi(sp) * v1, blo(sp) * v0);
      sX[(8 * c + j) * 32 + pX] = (j & 1) ? __builtin_amdgcn_perm(xr[1][j >> 1], xr[0][j >> 1], 0x07060302u) : __builtin_amdgcn_perm(xr[1][j >> 1], xr[0][j >> 1], 0x05040100u);
    }
    ST(1);
    __syncthreads();
#pragma unroll
    for (int i = 0; i < 2; ++i) {
      const int piece = tid + 256 * i, ch = piece >> 3, g = piece & 7, p = ch * 32 + 4 * (g ^ (ch >> 3));
      st128<P_K0>(mkrsrc(ws, WS_END), WS_URT + ((64 * cb + ch) * S + (1984 - 64 * tb) + 8 * g) * 2, *(const u32x4*)(sU + p));
      st128<P_K0>(mkrsrc(ws, WS_END), WS_XT + ((64 * cb + ch) * S + 64 * tb + 8 * g) * 2, *(const u32x4*)(sX + p));
    }
  } else {
    const int sb = b - 320, row = 64 * ((sb & 7) + 8 * (sb >> 5)) + 16 * ((sb >> 3) & 3) + (tid >> 4), s = tid & 15;
    u32x4 r[5];
#pragma unroll
    for (int i = 0; i < 5; ++i) r[i] = ld128nt(rx, (row * D + 8 * (s + 16 * i)) * 2);
    pin();
    float sum = 0.f;
#pragma unroll
    for (int i = 0; i < 5; ++i)
#pragma unroll
      for (int j = 0; j < 4; ++j) sum += blo(r[i][j]) + bhi(r[i][j]);
#pragma unroll
    for (int o = 1; o < 16; o <<= 1) sum += __shfl_xor(sum, o);
    const float mean = sum * (1.f / D);
    float q = 0.f;
#pragma unroll
    for (int i = 0; i < 5; ++i)
#pragma unroll
      for (int j = 0; j < 4; ++j) { const float a = blo(r[i][j]) - mean, c2 = bhi(r[i][j]) - mean; q += a * a + c2 * c2; }
#pragma unroll
    for (int o = 1; o < 16; o <<= 1) q += __shfl_xor(q, o);
    const float sd = __builtin_amdgcn_sqrtf(rbf(rbf(q * (1.f / D)) + EPS));
    if (s == 0) ((u32*)(ws + WS_ST1))[row] = pk2(sd, mean);
  }
  ST(2); STAMP_OUT(0);
}

constexpr int NW1 = 112;
constexpr int NTASK2 = 540, TPW = 2, NW2 = (NTASK2 + 4 * TPW - 1) / (4 * TPW);
constexpr int KC_STRIDE = 4160;
constexpr int K1_U = 32 * 1024, K1_LDS = K1_U + 2304 * 2;

DI void w1_strip(int r, const u16* wg, const u16* wu, char* ws, char* sm) {
  const int p = 8 * (r >> 4) + (r & 7), s = 2 * p + ((r >> 3) & 1);
  if (s >= 108) return;
  const int tid = threadIdx.x, lane = tid & 63, w = __builtin_amdgcn_readfirstlane(tid >> 6);
  const int m = s / 54, col0 = 32 * (s % 54), c4 = tid & 3, kg = tid >> 2;
  const rsrc_t rw = mkrsrc(m ? wu : wg, D * F * 2), rs = mkrsrc(ws, WS_END);
  u32x4 rr[10];
#pragma unroll
  for (int i = 0; i < 10; ++i) rr[i] = ld128nt(rw, ((kg + 64 * i) * F + col0 + 8 * c4) * 2);
  pin();
  const int g = lane >> 4, qp = (lane & 15) >> 2, p4 = lane & 3, c2 = w & 1;
  const u32x4 ones = {0x3f803f80u, 0x3f803f80u, 0x3f803f80u, 0x3f803f80u};
  u32x4 fr[10];
  f32x4 cs = {0, 0, 0, 0};
#pragma unroll
  for (int hf = 0; hf < 2; ++hf) {
    if (hf) __syncthreads();
#pragma unroll
    for (int i = 0; i < 5; ++i) *(u32x4*)(sm + (kg + 64 * i) * 80 + 16 * c4) = rr[5 * hf + i];
    __syncthreads();
#pragma unroll
    for (int i = 0; i < 5; ++i) {
      const char* im = sm + (32 * ((w >> 1) + 2 * i) + 8 * g + qp) * 80 + 32 * c2 + 8 * p4;
      fr[5 * hf + i] = tr_frag(im, im + 4 * 80);
      cs = mfma16(ones, fr[5 * hf + i], cs);
    }
  }
  float* red = (float*)(sm + 320 * 80);
  if (lane < 16) red[w * 16 + lane] = cs[0];
  __syncthreads();
  const float mu = (red[w * 16 + (lane & 15)] + red[(w ^ 2) * 16 + (lane & 15)]) * (1.f / D);
  const int ft = (col0 >> 4) + c2;
  const int c = ft < 84 ? ft / 7 : 12 + (ft - 84) / 6, pair = ft < 84 ? ft % 7 : (ft - 84) % 6;
#pragma unroll
  for (int i = 0; i < 10; ++i) {
    const int ks = 10 * (i / 5) + (w >> 1) + 2 * (i % 5);
    const u32x4 a = fr[i];
    st128<P_W>(rs, WS_W1T + ((c * 20 + ks) * 14 + 2 * pair + m) * 1024 + lane * 16,
          u32x4{pk2(blo(a[0]) - mu, bhi(a[0]) - mu), pk2(blo(a[1]) - mu, bhi(a[1]) - mu), pk2(blo(a[2]) - mu, bhi(a[2]) - mu), pk2(blo(a[3]) - mu, bhi(a[3]) - mu)});
  }
}
DI void w2_tasks(int pb, const u16* wd, char* ws, char* sm) {
  const int tid = threadIdx.x, lane = tid & 63, w = __builtin_amdgcn_readfirstlane(tid >> 6);
  const rsrc_t rw = mkrsrc(wd, D * F * 2), rs = mkrsrc(ws, WS_END);
  char* im = sm + w * (32 * 144);
  u32x4 ld[TPW][4];
  int task[TPW];
#pragma unroll
  for (int u = 0; u < TPW; ++u) {
    int t = (pb * 4 + w) * TPW + u; if (t >= NTASK2) t = NTASK2 - 1;
    task[u] = t;
    const int ks = t / 10, dr = t % 10;
    int lo, hi;
    if (ks < 36) { lo = 7 * (ks / 3) + 2 * (ks % 3); hi = lo + 1; } else if (ks < 48) { lo = 84 + 2 * (ks - 36); hi = lo + 1; } else { lo = 14 * (ks - 48) + 6; hi = lo + 7; }
#pragma unroll
    for (int i = 0; i < 4; ++i) { const int r = (lane >> 3) + 8 * i; ld[u][i] = ld128nt(rw, ((16 * (r < 16 ? lo : hi) + (r & 15)) * D + 64 * dr + 8 * (lane & 7)) * 2); }
  }
  pin();
  const int g = lane >> 4, qp = (lane & 15) >> 2, p4 = lane & 3;
#pragma unroll
  for (int u = 0; u < TPW; ++u) {
#pragma unroll
    for (int i = 0; i < 4; ++i) *(u32x4*)(im + ((lane >> 3) + 8 * i) * 144 + 16 * (lane & 7)) = ld[u][i];
    const int ks = task[u] / 10;
#pragma unroll
    for (int c4 = 0; c4 < 4; ++c4) {
      const int dt = 4 * (task[u] % 10) + c4;
      const u32x4 fr = tr_frag(im + (4 * g + qp) * 144 + 32 * c4 + 8 * p4, im + (16 + 4 * g + qp) * 144 + 32 * c4 + 8 * p4);
      st128<P_W>(rs, WS_W2T + (((dt / 5) * 54 + ks) * 5 + dt % 5) * 1024 + lane * 16, fr);
    }
  }
}

__global__ __launch_bounds__(256, 4) void k1_conv(const u16* __restrict__ ssmk, const u16* __restrict__ ssmD, const u16* __restrict__ wg, const u16* __restrict__ wu, const u16* __restrict__ wd, char* __restrict__ ws) {
  extern __shared__ __attribute__((aligned(16))) char sm[];
  const int b = blockIdx.x, tid = threadIdx.x;
  STAMP_DECL ST(0);
  if (b >= D + NW1) { w2_tasks(b - D - NW1, wd, ws, sm); ST(5); STAMP_OUT(1); return; }
  if (b >= D) { w1_strip(b - D, wg, wu, ws, sm); ST(5); STAMP_OUT(1); return; }
  const int d = b, lane = tid & 63, w = __builtin_amdgcn_readfirstlane(tid >> 6);
  const int l15 = lane & 15, q = lane >> 4;
  const rsrc_t rk = mkrsrc(ssmk + d * S, S * 2), rs = mkrsrc(ws, WS_END);
  const u32x4 c0 = ld128nt(rk, 16 * tid), c1 = ld128nt(rk, 16 * tid + 16);
  const u32x4 uu = ld128(rs, WS_URT + d * (S * 2) + 16 * tid);
  u32x2 xv[2]; u32x4 st[2];
#pragma unroll
  for (int i = 0; i < 2; ++i) {
    const int t = 256 * (2 * w + i) + 16 * l15 + 4 * q;
    xv[i] = ld64(rs, WS_XT + (d * S + t) * 2);
    st[i] = ld128(rs, WS_ST1 + t * 4);
  }
  const float Dd = blo(ssmD[d]);
  pin();
  ST(1);
  auto build = [&](int m, const u32x4& a, const u32x4& bb) {
    const u32 ww[8] = {a[0], a[1], a[2], a[3], bb[0], bb[1], bb[2], bb[3]};
    u32x4 o0, o1, o2, o3;
#pragma unroll
    for (int i = 0; i < 4; ++i) {
      o0[i] = __builtin_amdgcn_alignbyte(ww[i + 1], ww[i], 2); o1[i] = ww[i + 1];
      o2[i] = __builtin_amdgcn_alignbyte(ww[i + 2], ww[i + 1], 2); o3[i] = ww[i + 2];
    }
    *(u32x4*)(sm + 0 * KC_STRIDE + 16 * m) = o0; *(u32x4*)(sm + 1 * KC_STRIDE + 16 * m) = o1;
    *(u32x4*)(sm + 2 * KC_STRIDE + 16 * m) = o2; *(u32x4*)(sm + 3 * KC_STRIDE + 16 * m) = o3;
  };
  const u32x4 zero4 = {0, 0, 0, 0};
  build(tid + 2, c0, c1);
  if (tid < 2) build(1 - tid, zero4, tid == 0 ? c0 : zero4);
  *(u32x4*)(sm + K1_U + 16 * tid) = uu;
  if (tid < 32) *(u32x4*)(sm + K1_U + 4096 + 16 * tid) = zero4;
  __syncthreads();
  ST(2);
  f32x4 acc[8];
#pragma unroll
  for (int j = 0; j < 8; ++j) acc[j] = f32x4{0, 0, 0, 0};
#pragma unroll
  for (int rr = 0; rr < 2; ++rr) {
    const int rho = 2 * w + rr;
    const char* pf = sm + (l15 & 3) * KC_STRIDE + 2 * (l15 & 12) + 16 * q + 64 * rho;
    const char* pg = sm + K1_U + 480 - 32 * l15 + 64 * rho + 16 * q;
    u32x4 Ff[8], Gf[8];
#pragma unroll
    for (int i = 0; i < 8; ++i) {
      Gf[i] = *(const u32x4*)(pg + 512 * (7 - i));
      const u32x2 a = *(const volatile LAS u32x2*)(pf + 512 * i), bb = *(const volatile LAS u32x2*)(pf + 512 * i + 8);
      Ff[i] = u32x4{a[0], a[1], bb[0], bb[1]};
    }
#pragma unroll
    for (int i = 0; i < 8; ++i)
#pragma unroll
      for (int e = 0; e + i < 8; ++e) acc[i + e] = mfma16(Ff[i], Gf[e], acc[i + e]);
  }
  ST(3);
  __syncthreads();
  f32x4 own[2];
  {
    char* p = sm + w * 1024 + lane * 16;
#define K1_PUT(W) do { _Pragma("unroll") for (int j = 0; j < 8; ++j) { if ((j >> 1) != (W)) *(f32x4*)(p + j * 4096) = acc[j]; } own[0] = acc[2 * (W)]; own[1] = acc[2 * (W) + 1]; } while (0)
    if (w == 0) K1_PUT(0); else if (w == 1) K1_PUT(1); else if (w == 2) K1_PUT(2); else K1_PUT(3);
  }
  __syncthreads();
  ST(4);
#pragma unroll
  for (int i = 0; i < 2; ++i) {
    const int j = 2 * w + i;
    f32x4 y = own[i];
#pragma unroll
    for (int s = 1; s < 4; ++s) y += *(const f32x4*)(sm + (j * 4 + ((w + s) & 3)) * 1024 + lane * 16);
    const int t = 256 * j + 16 * l15 + 4 * q;
    const u32x2 ur = *(const u32x2*)(sm + K1_U + 2 * (2044 - t));
    const float uv[4] = {bhi(ur[1]), blo(ur[1]), bhi(ur[0]), blo(ur[0])};
    const float xx[4] = {blo(xv[i][0]), bhi(xv[i][0]), blo(xv[i][1]), bhi(xv[i][1])};
    float h[4];
#pragma unroll
    for (int r = 0; r < 4; ++r) {
      const float ln = (xx[r] - bhi(st[i][r])) * __builtin_amdgcn_rcpf(blo(st[i][r]));
      h[r] = fmaf(ln, fmaf(uv[r], Dd, y[r]), xx[r]);
    }
    st64<P_HT>(rs, WS_HT + (d * S + t) * 2, u32x2{pk2(h[0], h[1]), pk2(h[2], h[3])});
  }
  ST(5); STAMP_OUT(1);
}

template <int N> DI void waitv() { asm volatile("s_waitcnt vmcnt(%0)" ::"n"(N) : "memory"); }
DI void dma16(u32x4 srd, u32 lds, u32 voff, u32 soff) {
  asm volatile("s_mov_b32 m0, %0\n\ts_nop 0\n\tbuffer_load_dwordx4 %1, %2, %3 offen lds" ::"s"(lds), "v"(voff), "s"(srd), "s"(soff) : "memory");
}
DI u32x4 mksrd(const void* p, u32 bytes) { const unsigned long long a = (unsigned long long)p; return u32x4{(u32)a, (u32)(a >> 32) & 0xffffu, bytes, 0x00020000u}; }
DI u32 ldsaddr(const char* p) { return (u32)(unsigned long long)(LAS const char*)p; }
DI void bar() { __builtin_amdgcn_s_barrier(); }

template <int CNT, int K> DI void tail_waits() { waitv<CNT * K>(); bar(); if constexpr (K > 0) tail_waits<CNT, K - 1>(); }
template <int CNT, int NS, int NST, int SLOT, int SPLIT = NS, int OFF0 = 0, int OFF1 = 0>
struct Loader {
  u32x4 srd; u32 lb; u32 voff[CNT], sstep[CNT], ldst[CNT]; int rot;
  static DI u32 slot_off(int j) { return (u32)(j * SLOT + (j < SPLIT ? OFF0 : OFF1)); }
  DI void issue(int st, int j) {
    int sr = st + rot; if (sr >= NST) sr -= NST;
    const u32 slot = slot_off(j);
#pragma unroll
    for (int i = 0; i < CNT; ++i) dma16(srd, lb + slot + ldst[i], voff[i], sr * sstep[i]);
  }
  template <int S0, int S1> DI void prologue() {
#pragma unroll
    for (int s = S0; s < S1; ++s) issue(s, s);
  }
  DI void run() {
    static_assert(CNT * (NS - 3) <= 63, "vmcnt");
    waitv<CNT * (NS - 3)>();
    bar();
    issue(NS - 2, NS - 2);
    int j = NS - 1;
    for (int n = 0; n + NS - 1 < NST; ++n) {
      waitv<CNT * (NS - 3)>();
      bar();
      issue(n + NS - 1, j);
      j = j == NS - 1 ? 0 : j + 1;
    }
    tail_waits<CNT, NS - 3>();
  }
};

constexpr int G1_NS = 6, G1_SLOT = 22 * 1024, G1_NST = 20;
#define G2NS 12
constexpr int G2_NS = G2NS, G2_SLOT = 10 * 1024, G2_RING = 2 * G1_SLOT, G2_NST = 27;
DI constexpr int g2_off(int j) { return j < 8 ? G2_RING + j * G2_SLOT : (j - 8) * G2_SLOT; }
#define G2HW 9
constexpr int G2_HW = G2HW;
constexpr int G_AUX = G1_NS * G1_SLOT, G_LDS = G_AUX + 11 * 1024;
static_assert(G2_RING + 8 * G2_SLOT <= G_AUX && (G2_NS - 8) * G2_SLOT <= G2_RING, "phase B ring lies in the slots that phase A has left");
typedef __attribute__((address_space(1))) u32 gu32;

template <int CNTA, int CNTB>
DI void mlp_loader(char* sm, char* ws, int rt, int c, int rt3, int dg, int rotA, int rotB, int wi, int lw, int lane) {
  const u32x4 srd = mksrd(ws, WS_END);
  const u32 lb = ldsaddr(sm);
  {
#pragma unroll
    for (int i = 0; i < 3; ++i) {
      const int p = lw + 4 * i;
      if (p < 10) dma16(srd, lb + G_AUX + p * 1024, WS_HT + (80 * dg + 8 * p + (lane >> 3)) * (S * 2) + rt3 * 128 + (lane & 7) * 16, 0);
    }
    Loader<CNTA, G1_NS, G1_NST, G1_SLOT> L;
    L.srd = srd; L.lb = lb; L.rot = rotA;
    const int l15 = lane & 15, q = lane >> 4;
#pragma unroll
    for (int i = 0; i < CNTA; ++i) {
      const int p = lw + 4 * i;
      L.ldst[i] = p * 1024;
      if (p < 14) { L.voff[i] = WS_W1T + (c * 20 * 14 + p) * 1024 + lane * 16; L.sstep[i] = 14 * 1024; }
      else { const int kk = 4 * (p - 14) + q, f = (kk & 3) | (((kk >> 3) & 1) << 2);
             L.voff[i] = WS_HT + kk * (S * 2) + rt * 256 + 16 * (l15 ^ (f << 1)); L.sstep[i] = 32 * S * 2; }
    }
    L.template prologue<0, G1_NS - 2>();
    L.run();
  }
  Loader<CNTB, G2_NS, G2_NST, G2_SLOT, 8, G2_RING, -8 * G2_SLOT> L;
  L.srd = srd; L.lb = lb; L.rot = rotB;
#pragma unroll
  for (int i = 0; i < CNTB; ++i) { const int p = lw + 4 * i; L.ldst[i] = p * 1024; L.voff[i] = WS_W2T + (dg * 54 * 5 + p) * 1024 + lane * 16; L.sstep[i] = 10 * 1024;
  }
  L.template prologue<0, (G2_NS - 2 < 8 ? G2_NS - 2 : 8)>();
  bar();
  L.template prologue<8, G2_NS - 2>();
  L.run();
  __syncthreads();
  __syncthreads();
}

template <int NT, int T0, bool STAT>
DI void g1_body(char* sm, char* ws, int rt, int c, int w, int lane) {
  const int tw = w & 3, l15 = lane & 15, q = lane >> 4;
  const int qp = l15 >> 2, p4 = lane & 3, msk = 2 * qp | ((q & 1) << 3);
  int hoff[2];
#pragma unroll
  for (int i = 0; i < 2; ++i) hoff[i] = 14336 + (8 * q + qp) * 256 + 16 * ((4 * tw + 2 * i + (p4 >> 1)) ^ msk) + 8 * (p4 & 1);
  const int woff = T0 * 1024 + lane * 16;
  u32x4 W[NT];
  u32x2 h0a, h0b, h1a, h1b;
  f32x4 acc[2][NT], accS[2], accG[2];
#pragma unroll
  for (int i = 0; i < 2; ++i) {
    accS[i] = f32x4{0, 0, 0, 0}; accG[i] = f32x4{0, 0, 0, 0};
#pragma unroll
    for (int m = 0; m < NT; ++m) acc[i][m] = f32x4{0, 0, 0, 0};
  }
  const u32x4 ones = {0x3f803f80u, 0x3f803f80u, 0x3f803f80u, 0x3f803f80u};
  auto half0 = [&](const char* slot) {
    const u32x4 h0 = {h0a[0], h0a[1], h0b[0], h0b[1]};
#pragma unroll
    for (int m = 0; m < NT; ++m) {
      acc[0][m] = mfma16(W[m], h0, acc[0][m]);
      if (m == 0) h1a = trd(slot + hoff[1]);
      if (m == 1) h1b = trd(slot + hoff[1] + 1024);
      pin();
    }
    if (STAT) { accS[0] = mfma16(ones, h0, accS[0]); accG[0] = mfma16(h0, h0, accG[0]); pin(); }
  };
  auto half1 = [&](const char* nslot, bool doread) {
    const u32x4 h1 = {h1a[0], h1a[1], h1b[0], h1b[1]};
#pragma unroll
    for (int m = 0; m < NT; ++m) {
      acc[1][m] = mfma16(W[m], h1, acc[1][m]);
      if (doread) {
        W[m] = *(const u32x4*)(nslot + woff + m * 1024);
        if (m == 0) h0a = trd(nslot + hoff[0]);
        if (m == 1) h0b = trd(nslot + hoff[0] + 1024);
      }
      pin();
    }
    if (STAT) { accS[1] = mfma16(ones, h1, accS[1]); accG[1] = mfma16(h1, h1, accG[1]); pin(); }
  };
  bar();
#pragma unroll
  for (int m = 0; m < NT; ++m) {
    W[m] = *(const u32x4*)(sm + woff + m * 1024);
    if (m == 0) h0a = trd(sm + hoff[0]);
    if (m == 1) h0b = trd(sm + hoff[0] + 1024);
  }
  u32 cur = 0;
  half0(sm);
  for (int n = 0; n < G1_NST - 1; ++n) {
    bar();
    cur = cur == (G1_NS - 1) * G1_SLOT ? 0u : cur + G1_SLOT;
    half1(sm + cur, true);
    half0(sm + cur);
  }
  half1(sm, false);
  const rsrc_t rs = mkrsrc(ws, WS_END);
  const int ksb = 3 * c;
#pragma unroll
  for (int i = 0; i < 2; ++i) {
    const u32 ttile = 8 * rt + 2 * tw + i;
    u32 o[NT / 2][2];
#pragma unroll
    for (int pr = 0; pr < NT / 2; ++pr) {
      const f32x4 g = acc[i][2 * pr], u = acc[i][2 * pr + 1];
      o[pr][0] = pk2(fmaxf(g[0], 0.f) * u[0], fmaxf(g[1], 0.f) * u[1]); o[pr][1] = pk2(fmaxf(g[2], 0.f) * u[2], fmaxf(g[3], 0.f) * u[3]);
    }
    if (NT == 8) {
      st128(rs, WS_HM + ((ttile * 54 + ksb) * 64 + lane) * 16, u32x4{o[0][0], o[0][1], o[1][0], o[1][1]});
      st128(rs, WS_HM + ((ttile * 54 + ksb + 1) * 64 + lane) * 16, u32x4{o[2 % (NT / 2)][0], o[2 % (NT / 2)][1], o[3 % (NT / 2)][0], o[3 % (NT / 2)][1]});
    } else {
      st128(rs, WS_HM + ((ttile * 54 + ksb + 2) * 64 + lane) * 16, u32x4{o[0][0], o[0][1], o[1][0], o[1][1]});
      if (NT == 6) st64(rs, WS_HM + ((ttile * 54 + 48 + (c >> 1)) * 64 + lane) * 16 + 8 * (c & 1), u32x2{o[2 % (NT / 2)][0], o[2 % (NT / 2)][1]});
    }
    if (STAT) {
      if (tw == c - 12 && q == (l15 >> 2)) {
        const int r = l15 & 3;
        const float s2 = r == 0 ? accG[i][0] : r == 1 ? accG[i][1] : r == 2 ? accG[i][2] : accG[i][3];
        const float mean = accS[i][0] * (1.f / D), var = fmaxf(s2 * (1.f / D) - mean * mean, 0.f);
        const float rinv = __builtin_amdgcn_rcpf(rbf(__builtin_amdgcn_sqrtf(rbf(rbf(var) + EPS))));
        __builtin_amdgcn_raw_buffer_store_b32(__float_as_uint(rinv * rinv), rs, WS_R2 + (16 * ttile + l15) * 4, 0, AUX_WT);
      }
    }
  }
}

DI void g2_body(char* sm, char* ws, u16* out, int rt3, int dg, int rot, int w, int lane STAMP_PARAM) {
  const int tt = w & 3, kh = w >> 2, l15 = lane & 15, q = lane >> 4;
  const rsrc_t rs = mkrsrc(ws, WS_END);
  const u32 hbase = WS_HM + ((4 * rt3 + tt) * 54 + kh) * 1024 + lane * 16;
  u32x4 H[G2_HW];
  auto hload = [&](int n) { int sr = n + rot; if (sr >= G2_NST) sr -= G2_NST;
    return __builtin_amdgcn_raw_buffer_load_b128(rs, hbase, sr * 2048, AUX_WT);
  };
#pragma unroll
  for (int j = 0; j < G2_HW; ++j) H[j] = hload(j);
  const float r2 = __uint_as_float(__builtin_amdgcn_raw_buffer_load_b32(rs, WS_R2 + (64 * rt3 + 16 * tt + l15) * 4, 0, AUX_WT));
  pin();
  const char* wb = sm + kh * 5 * 1024 + lane * 16;
  u32x4 Wa[5], Wb[5];
  f32x4 acc[5];
#pragma unroll
  for (int m = 0; m < 5; ++m) { acc[m] = f32x4{0, 0, 0, 0}; Wa[m] = *(const u32x4*)(wb + g2_off(0) + m * 1024); }
  auto step = [&](u32x4 (&Wc)[5], u32x4 (&Wn)[5], int n) {
    if (n + 1 < G2_NST) bar();
#pragma unroll
    for (int m = 0; m < 5; ++m) {
      if (n + 1 < G2_NST) Wn[m] = *(const u32x4*)(wb + g2_off((n + 1) % G2_NS) + m * 1024);
      acc[m] = mfma16(Wc[m], H[n % G2_HW], acc[m]);
      pin();
    }
    if (n + G2_HW < G2_NST) { H[n % G2_HW] = hload(n + G2_HW); pin(); }
  };
#pragma unroll
  for (int n = 0; n < G2_NST; ++n) { if (n & 1) step(Wb, Wa, n); else step(Wa, Wb, n);
  }
  ST(4);
  __syncthreads();
  if (kh == 1) {
#pragma unroll
    for (int m = 0; m < 5; ++m) *(f32x4*)(sm + (tt * 5 + m) * 1024 + lane * 16) = acc[m];
  }
  __syncthreads();
  if (kh == 0) {
    const u16* res = (const u16*)(sm + G_AUX);
#pragma unroll
    for (int m = 0; m < 5; ++m) {
      const f32x4 o = acc[m] + *(const f32x4*)(sm + (tt * 5 + m) * 1024 + lane * 16);
      float r[4];
#pragma unroll
      for (int j = 0; j < 4; ++j) r[j] = __uint_as_float((u32)res[(16 * m + 4 * q + j) * 64 + 16 * tt + l15] << 16) + rbf(o[j] * r2);
      *(u32x2*)(out + (64 * rt3 + 16 * tt + l15) * D + 80 * dg + 16 * m + 4 * q) = u32x2{pk2(r[0], r[1]), pk2(r[2], r[3])};
    }
  }
}

DI void mlp_warm(char* sm, char* ws, int dg, int i, int lane) {
  const u32x4 srd = mksrd(ws, WS_END);
  const u32 lb = ldsaddr(sm) + G_AUX + 10 * 1024;
  const u32 voff = WS_W2T + (dg >> 2) * (4 * 54 * 5 * 1024) + i * 34560 + lane * 16;
  bar();
  for (int n = 0; n < 17; ++n) { bar(); dma16(srd, lb, voff, n * 2048); dma16(srd, lb, voff, n * 2048 + 1024); }
  waitv<0>();
}
constexpr int MLP_THREADS = 768;
__global__ __launch_bounds__(MLP_THREADS) void k2_mlp(char* __restrict__ ws, u16* __restrict__ out) {
  extern __shared__ __attribute__((aligned(16))) char sm[];
  const int b = blockIdx.x, x = b & 7, i = b >> 3, cq = x >> 1;
  const int rt = 8 * (x & 1) + (i & 7), c = 4 * cq + (i >> 3);
  const int rt3 = 2 * rt + (cq >> 1), dg = 4 * (cq & 1) + (c & 3);
  const int lane = threadIdx.x & 63, w = __builtin_amdgcn_readfirstlane(threadIdx.x >> 6);
#define ROTA 0
#define ROTB 0
  const int rotA = ROTA ? (5 * (c & 3) + (5 * (i & 7)) / 2) % G1_NST : 0, rotB = ROTB ? (7 * (dg & 3) + (27 * (i & 7)) / 8) % G2_NST : 0;
  if (w == 12) { mlp_warm(sm, ws, dg, i, lane); return; }
  if (w >= 8) {
    if (w < 10) mlp_loader<6, 3>(sm, ws, rt, c, rt3, dg, rotA, rotB, i, w - 8, lane); else mlp_loader<5, 2>(sm, ws, rt, c, rt3, dg, rotA, rotB, i, w - 8, lane);
    return;
  }
  STAMP_DECL ST(0);
  if (w < 4) g1_body<8, 0, false>(sm, ws, rt, c, w, lane);
  else if (c < 12) g1_body<6, 8, false>(sm, ws, rt, c, w, lane);
  else g1_body<4, 8, true>(sm, ws, rt, c, w, lane);
  ST(1);
  asm volatile("s_waitcnt vmcnt(0)" ::: "memory");
  bar();
  ST(2);
  if (threadIdx.x == 0) {
    gu32* cnt = (gu32*)(ws + WS_CNT) + (rt3 >> 1) * 32;
    __hip_atomic_fetch_add((gu32*)(ws + WS_CNT) + rt * 32, 1u, __ATOMIC_RELAXED, __HIP_MEMORY_SCOPE_AGENT);
    for (u32 spins = 0; __hip_atomic_load(cnt, __ATOMIC_RELAXED, __HIP_MEMORY_SCOPE_AGENT) < 16u && spins < (1u << 20); ++spins) __builtin_amdgcn_s_sleep(1);
  }
  __builtin_amdgcn_fence(__ATOMIC_ACQUIRE, "wavefront");
  bar();
  ST(3);
  g2_body(sm, ws, out, rt3, dg, rotB, w, lane STAMP_PASS);
  ST(5); STAMP_OUT(2);
}

extern "C" void kernel_launch(void* const* d_in, const int* in_sizes, int n_in, void* d_out, int out_size, void* d_ws, size_t ws_size, hipStream_t stream) {
  const u16* q = (const u16*)d_in[0]; const u16* k = (const u16*)d_in[1]; const u16* v = (const u16*)d_in[2];
  const u16* shk = (const u16*)d_in[3]; const u16* ssmk = (const u16*)d_in[4]; const u16* ssmD = (const u16*)d_in[5];
  const u16* wg = (const u16*)d_in[7]; const u16* wu = (const u16*)d_in[8]; const u16* wd = (const u16*)d_in[9];
  char* ws = (char*)d_ws + ((ws_size - WS_END - 4096) & ~(size_t)0x1fffff);
  (void)hipFuncSetAttribute((const void*)k2_mlp, hipFuncAttributeMaxDynamicSharedMemorySize, G_LDS);
  k0_prep<<<448, 256, 0, stream>>>(q, k, v, shk, ws);
  k1_conv<<<D + NW1 + NW2, 256, K1_LDS, stream>>>(ssmk, ssmD, wg, wu, wd, ws);
  k2_mlp<<<256, MLP_THREADS, G_LDS, stream>>>(ws, (u16*)d_out);
}
```

```cpp
#include <hip/hip_runtime.h>
#include <stdint.h>
#define NO_WARM 1

typedef unsigned short u16;
typedef unsigned int u32;
typedef u32 u32x2 __attribute__((ext_vector_type(2)));
typedef u32 u32x4 __attribute__((ext_vector_type(4)));
typedef short s16x4 __attribute__((ext_vector_type(4)));
typedef short s16x8 __attribute__((ext_vector_type(8)));
typedef float f32x2 __attribute__((ext_vector_type(2)));
typedef float f32x4 __attribute__((ext_vector_type(4)));
typedef __bf16 bf16x2 __attribute__((ext_vector_type(2)));
#define LAS __attribute__((address_space(3)))
#define DI __device__ __forceinline__

constexpr int S = 2048, D = 640, F = 1728;
constexpr float EPS = 1e-5f;

constexpr u32 WS_URT = 0;
constexpr u32 WS_XT  = WS_URT + D * S * 2;
constexpr u32 WS_HT  = WS_XT + D * S * 2;
constexpr u32 WS_ST1 = WS_HT + D * S * 2;
constexpr u32 WS_R2  = WS_ST1 + S * 4;
constexpr u32 WS_CNT = WS_R2 + S * 4;
constexpr u32 WS_W1T = WS_CNT + 16384;
constexpr u32 WS_W2T = WS_W1T + 16 * 20 * 14 * 1024;
constexpr u32 WS_HM  = WS_W2T + 8 * 54 * 5 * 1024;
constexpr u32 WS_END = WS_HM + 128 * 54 * 1024;

typedef __amdgpu_buffer_rsrc_t rsrc_t;
DI float blo(u32 u) { return __uint_as_float(u << 16); }
DI float bhi(u32 u) { return __uint_as_float(u & 0xffff0000u); }
DI u32 pk2(float a, float b) { bf16x2 r; r[0] = (__bf16)a; r[1] = (__bf16)b; return __builtin_bit_cast(u32, r); }
DI float rbf(float a) { return blo(pk2(a, a)); }
DI f32x4 mfma16(u32x4 a, u32x4 b, f32x4 c) {
  return __builtin_amdgcn_mfma_f32_16x16x32_bf16(__builtin_bit_cast(s16x8, a), __builtin_bit_cast(s16x8, b), c, 0, 0, 0);
}
DI rsrc_t mkrsrc(const void* p, u32 bytes) { return __builtin_amdgcn_make_buffer_rsrc((void*)p, 0, bytes, 0x00020000); }
constexpr int AUX_WT = 16, AUX_NT = 2;
DI u32x4 ld128nt(rsrc_t rs, u32 off) { return __builtin_amdgcn_raw_buffer_load_b128(rs, off, 0, AUX_NT); }
DI u32x4 ld128(rsrc_t rs, u32 off) { return __builtin_amdgcn_raw_buffer_load_b128(rs, off, 0, 0); }
DI u32x2 ld64(rsrc_t rs, u32 off) { return __builtin_amdgcn_raw_buffer_load_b64(rs, off, 0, 0); }
template <int AUX = AUX_WT> DI void st128(rsrc_t rs, u32 off, u32x4 v) { __builtin_amdgcn_raw_buffer_store_b128(v, rs, off, 0, AUX); }
template <int AUX = AUX_WT> DI void st64(rsrc_t rs, u32 off, u32x2 v) { __builtin_amdgcn_raw_buffer_store_b64(v, rs, off, 0, AUX); }
constexpr int P_HT = 0, P_K0 = 0, P_W = 16;
DI void pin() { __builtin_amdgcn_sched_barrier(0); }
DI u32x2 trd(const char* p) { return __builtin_bit_cast(u32x2, __builtin_amdgcn_ds_read_tr16_b64_v4i16((LAS s16x4*)p)); }
DI u32x4 tr_frag(const char* p0, const char* p1) { const u32x2 a = trd(p0), b = trd(p1); return u32x4{a[0], a[1], b[0], b[1]}; }

#define STAMP_DECL
#define STAMP_PARAM
#define STAMP_PASS
#define ST(i) do {} while (0)
#define STAMP_OUT(K) do {} while (0)

extern "C" __global__ __launch_bounds__(256) void k0_prep(const u16* __restrict__ x, const u16* __restrict__ k, const u16* __restrict__ v, const u16* __restrict__ shk, char* __restrict__ ws) {
  __shared__ __attribute__((aligned(16))) u32 sm[2 * 64 * 32];
  const int b = blockIdx.x, tid = threadIdx.x;
  STAMP_DECL ST(0);
  const rsrc_t rx = mkrsrc(x, S * D * 2);
  if (b == 447 && tid < 16) ((u32*)(ws + WS_CNT))[tid * 32] = 0u;
  if (b == 446) {
    unsigned long long c0, c1;
    asm volatile("s_getpc_b64 s[40:41]\n\ts_add_u32 s40, s40, k1_conv@rel32@lo+4\n\ts_addc_u32 s41, s41, k1_conv@rel32@hi+12\n\ts_mov_b64 %0, s[40:41]\n\t"
                 "s_getpc_b64 s[40:41]\n\ts_add_u32 s40, s40, k9_end@rel32@lo+4\n\ts_addc_u32 s41, s41, k9_end@rel32@hi+12\n\ts_mov_b64 %1, s[40:41]"
                 : "=s"(c0), "=s"(c1) :: "s40", "s41", "scc");
    const rsrc_t rc = mkrsrc((const void*)c0, (u32)(c1 - c0));
    const u32 p0 = __builtin_amdgcn_raw_buffer_load_b32(rc, tid * 128, 0, 0), p1 = __builtin_amdgcn_raw_buffer_load_b32(rc, (tid + 256) * 128, 0, 0);
    asm volatile("" :: "v"(p0), "v"(p1));
  }
  if (b < 320) {
    const rsrc_t rk = mkrsrc(k, S * D * 2), rv = mkrsrc(v, S * D * 2), rw = mkrsrc(shk, D * 8);
    const int tb = (b & 7) + 8 * ((b >> 3) / 10), cb = (b >> 3) % 10, c = tid & 7, rg = tid >> 3;
    const int t = 64 * tb + 2 * rg, ch0 = 64 * cb + 8 * c;
    const u32 o = (u32)(t * D + ch0) * 2u;
    u32x4 kr[5], vr[2], xr[2], w[4];
#pragma unroll
    for (int i = 0; i < 5; ++i) kr[i] = ld128nt(rk, o + (u32)((i - 3) * D * 2));
#pragma unroll
    for (int i = 0; i < 2; ++i) { vr[i] = ld128nt(rv, o + i * D * 2); xr[i] = ld128nt(rx, o + i * D * 2); }
#pragma unroll
    for (int i = 0; i < 4; ++i) w[i] = ld128(rw, ch0 * 8 + 16 * i);
    pin();
    u32* sU = sm; u32* sX = sm + 64 * 32;
    const int colU = 31 - rg, colX = rg;
    const int pU = 4 * ((colU >> 2) ^ c) + (colU & 3), pX = 4 * ((colX >> 2) ^ c) + (colX & 3);
#pragma unroll
    for (int j = 0; j < 8; ++j) {
      float kk[5];
#pragma unroll
      for (int i = 0; i < 5; ++i) kk[i] = (j & 1) ? bhi(kr[i][j >> 1]) : blo(kr[i][j >> 1]);
      const u32 wa = w[j >> 1][(j & 1) * 2], wb = w[j >> 1][(j & 1) * 2 + 1];
      const float w0 = blo(wa), w1 = bhi(wa), w2 = blo(wb), w3 = bhi(wb);
      const float s0 = kk[0] * w3 + kk[1] * w2 + kk[2] * w1 + kk[3] * w0;
      const float s1 = kk[1] * w3 + kk[2] * w2 + kk[3] * w1 + kk[4] * w0;
      const u32 sp = pk2(s0, s1);
      const float v0 = (j & 1) ? bhi(vr[0][j >> 1]) : blo(vr[0][j >> 1]);
      const float v1 = (j & 1) ? bhi(vr[1][j >> 1]) : blo(vr[1][j >> 1]);
      sU[(8 * c + j) * 32 + pU] = pk2(bhi(sp) * v1, blo(sp) * v0);
      sX[(8 * c + j) * 32 + pX] = (j & 1) ? __builtin_amdgcn_perm(xr[1][j >> 1], xr[0][j >> 1], 0x07060302u) : __builtin_amdgcn_perm(xr[1][j >> 1], xr[0][j >> 1], 0x05040100u);
    }
    ST(1);
    __syncthreads();
#pragma unroll
    for (int i = 0; i < 2; ++i) {
      const int piece = tid + 256 * i, ch = piece >> 3, g = piece & 7, p = ch * 32 + 4 * (g ^ (ch >> 3));
      st128<P_K0>(mkrsrc(ws, WS_END), WS_URT + ((64 * cb + ch) * S + (1984 - 64 * tb) + 8 * g) * 2, *(const u32x4*)(sU + p));
      st128<P_K0>(mkrsrc(ws, WS_END), WS_XT + ((64 * cb + ch) * S + 64 * tb + 8 * g) * 2, *(const u32x4*)(sX + p));
    }
  } else {
    const int sb = b - 320, row = 64 * ((sb & 7) + 8 * (sb >> 5)) + 16 * ((sb >> 3) & 3) + (tid >> 4), s = tid & 15;
    u32x4 r[5];
#pragma unroll
    for (int i = 0; i < 5; ++i) r[i] = ld128nt(rx, (row * D + 8 * (s + 16 * i)) * 2);
    pin();
    float sum = 0.f;
#pragma unroll
    for (int i = 0; i < 5; ++i)
#pragma unroll
      for (int j = 0; j < 4; ++j) sum += blo(r[i][j]) + bhi(r[i][j]);
#pragma unroll
    for (int o = 1; o < 16; o <<= 1) sum += __shfl_xor(sum, o);
    const float mean = sum * (1.f / D);
    float q = 0.f;
#pragma unroll
    for (int i = 0; i < 5; ++i)
#pragma unroll
      for (int j = 0; j < 4; ++j) { const float a = blo(r[i][j]) - mean, c2 = bhi(r[i][j]) - mean; q += a * a + c2 * c2; }
#pragma unroll
    for (int o = 1; o < 16; o <<= 1) q += __shfl_xor(q, o);
    const float sd = __builtin_amdgcn_sqrtf(rbf(rbf(q * (1.f / D)) + EPS));
    if (s == 0) ((u32*)(ws + WS_ST1))[row] = pk2(sd, mean);
  }
  ST(2); STAMP_OUT(0);
}

constexpr int NW1 = 112;
constexpr int NTASK2 = 540, TPW = 2, NW2 = (NTASK2 + 4 * TPW - 1) / (4 * TPW);
constexpr int KC_STRIDE = 4160;
constexpr int K1_U = 32 * 1024, K1_LDS = K1_U + 2304 * 2;

DI void w1_strip(int r, const u16* wg, const u16* wu, char* ws, char* sm) {
  const int p = 8 * (r >> 4) + (r & 7), s = 2 * p + ((r >> 3) & 1);
  if (s >= 108) return;
  const int tid = threadIdx.x, lane = tid & 63, w = __builtin_amdgcn_readfirstlane(tid >> 6);
  const int m = s / 54, col0 = 32 * (s % 54), c4 = tid & 3, kg = tid >> 2;
  const rsrc_t rw = mkrsrc(m ? wu : wg, D * F * 2), rs = mkrsrc(ws, WS_END);
  u32x4 rr[10];
#pragma unroll
  for (int i = 0; i < 10; ++i) rr[i] = ld128nt(rw, ((kg + 64 * i) * F + col0 + 8 * c4) * 2);
  pin();
  const int g = lane >> 4, qp = (lane & 15) >> 2, p4 = lane & 3, c2 = w & 1;
  const u32x4 ones = {0x3f803f80u, 0x3f803f80u, 0x3f803f80u, 0x3f803f80u};
  u32x4 fr[10];
  f32x4 cs = {0, 0, 0, 0};
#pragma unroll
  for (int hf = 0; hf < 2; ++hf) {
    if (hf) __syncthreads();
#pragma unroll
    for (int i = 0; i < 5; ++i) *(u32x4*)(sm + (kg + 64 * i) * 80 + 16 * c4) = rr[5 * hf + i];
    __syncthreads();
#pragma unroll
    for (int i = 0; i < 5; ++i) {
      const char* im = sm + (32 * ((w >> 1) + 2 * i) + 8 * g + qp) * 80 + 32 * c2 + 8 * p4;
      fr[5 * hf + i] = tr_frag(im, im + 4 * 80);
      cs = mfma16(ones, fr[5 * hf + i], cs);
    }
  }
  float* red = (float*)(sm + 320 * 80);
  if (lane < 16) red[w * 16 + lane] = cs[0];
  __syncthreads();
  const float mu = (red[w * 16 + (lane & 15)] + red[(w ^ 2) * 16 + (lane & 15)]) * (1.f / D);
  const int ft = (col0 >> 4) + c2;
  const int c = ft < 84 ? ft / 7 : 12 + (ft - 84) / 6, pair = ft < 84 ? ft % 7 : (ft - 84) % 6;
#pragma unroll
  for (int i = 0; i < 10; ++i) {
    const int ks = 10 * (i / 5) + (w >> 1) + 2 * (i % 5);
    const u32x4 a = fr[i];
    st128<P_W>(rs, WS_W1T + ((c * 20 + ks) * 14 + 2 * pair + m) * 1024 + lane * 16,
          u32x4{pk2(blo(a[0]) - mu, bhi(a[0]) - mu), pk2(blo(a[1]) - mu, bhi(a[1]) - mu), pk2(blo(a[2]) - mu, bhi(a[2]) - mu), pk2(blo(a[3]) - mu, bhi(a[3]) - mu)});
  }
}
DI void w2_tasks(int pb, const u16* wd, char* ws, char* sm) {
  const int tid = threadIdx.x, lane = tid & 63, w = __builtin_amdgcn_readfirstlane(tid >> 6);
  const rsrc_t rw = mkrsrc(wd, D * F * 2), rs = mkrsrc(ws, WS_END);
  char* im = sm + w * (32 * 144);
  u32x4 ld[TPW][4];
  int task[TPW];
#pragma unroll
  for (int u = 0; u < TPW; ++u) {
    int t = (pb * 4 + w) * TPW + u; if (t >= NTASK2) t = NTASK2 - 1;
    task[u] = t;
    const int ks = t / 10, dr = t % 10;
    int lo, hi;
    if (ks < 36) { lo = 7 * (ks / 3) + 2 * (ks % 3); hi = lo + 1; } else if (ks < 48) { lo = 84 + 2 * (ks - 36); hi = lo + 1; } else { lo = 14 * (ks - 48) + 6; hi = lo + 7; }
#pragma unroll
    for (int i = 0; i < 4; ++i) { const int r = (lane >> 3) + 8 * i; ld[u][i] = ld128nt(rw, ((16 * (r < 16 ? lo : hi) + (r & 15)) * D + 64 * dr + 8 * (lane & 7)) * 2); }
  }
  pin();
  const int g = lane >> 4, qp = (lane & 15) >> 2, p4 = lane & 3;
#pragma unroll
  for (int u = 0; u < TPW; ++u) {
#pragma unroll
    for (int i = 0; i < 4; ++i) *(u32x4*)(im + ((lane >> 3) + 8 * i) * 144 + 16 * (lane & 7)) = ld[u][i];
    const int ks = task[u] / 10;
#pragma unroll
    for (int c4 = 0; c4 < 4; ++c4) {
      const int dt = 4 * (task[u] % 10) + c4;
      const u32x4 fr = tr_frag(im + (4 * g + qp) * 144 + 32 * c4 + 8 * p4, im + (16 + 4 * g + qp) * 144 + 32 * c4 + 8 * p4);
      st128<P_W>(rs, WS_W2T + (((dt / 5) * 54 + ks) * 5 + dt % 5) * 1024 + lane * 16, fr);
    }
  }
}

extern "C" __global__ __launch_bounds__(256, 4) void k1_conv(const u16* __restrict__ ssmk, const u16* __restrict__ ssmD, const u16* __restrict__ wg, const u16* __restrict__ wu, const u16* __restrict__ wd, char* __restrict__ ws) {
  extern __shared__ __attribute__((aligned(16))) char sm[];
  const int b = blockIdx.x, tid = threadIdx.x;
  STAMP_DECL ST(0);
  if (b >= D + NW1) { w2_tasks(b - D - NW1, wd, ws, sm); ST(5); STAMP_OUT(1); return; }
  if (b >= D) { w1_strip(b - D, wg, wu, ws, sm); ST(5); STAMP_OUT(1); return; }
  const int d = b, lane = tid & 63, w = __builtin_amdgcn_readfirstlane(tid >> 6);
  const int l15 = lane & 15, q = lane >> 4;
  const rsrc_t rk = mkrsrc(ssmk + d * S, S * 2), rs = mkrsrc(ws, WS_END);
  const u32x4 c0 = ld128nt(rk, 16 * tid), c1 = ld128nt(rk, 16 * tid + 16);
  const u32x4 uu = ld128(rs, WS_URT + d * (S * 2) + 16 * tid);
  u32x2 xv[2]; u32x4 st[2];
#pragma unroll
  for (int i = 0; i < 2; ++i) {
    const int t = 256 * (2 * w + i) + 16 * l15 + 4 * q;
    xv[i] = ld64(rs, WS_XT + (d * S + t) * 2);
    st[i] = ld128(rs, WS_ST1 + t * 4);
  }
  const float Dd = blo(ssmD[d]);
  pin();
  ST(1);
  auto build = [&](int m, const u32x4& a, const u32x4& bb) {
    const u32 ww[8] = {a[0], a[1], a[2], a[3], bb[0], bb[1], bb[2], bb[3]};
    u32x4 o0, o1, o2, o3;
#pragma unroll
    for (int i = 0; i < 4; ++i) {
      o0[i] = __builtin_amdgcn_alignbyte(ww[i + 1], ww[i], 2); o1[i] = ww[i + 1];
      o2[i] = __builtin_amdgcn_alignbyte(ww[i + 2], ww[i + 1], 2); o3[i] = ww[i + 2];
    }
    *(u32x4*)(sm + 0 * KC_STRIDE + 16 * m) = o0; *(u32x4*)(sm + 1 * KC_STRIDE + 16 * m) = o1;
    *(u32x4*)(sm + 2 * KC_STRIDE + 16 * m) = o2; *(u32x4*)(sm + 3 * KC_STRIDE + 16 * m) = o3;
  };
  const u32x4 zero4 = {0, 0, 0, 0};
  build(tid + 2, c0, c1);
  if (tid < 2) build(1 - tid, zero4, tid == 0 ? c0 : zero4);
  *(u32x4*)(sm + K1_U + 16 * tid) = uu;
  if (tid < 32) *(u32x4*)(sm + K1_U + 4096 + 16 * tid) = zero4;
  __syncthreads();
  ST(2);
  f32x4 acc[8];
#pragma unroll
  for (int j = 0; j < 8; ++j) acc[j] = f32x4{0, 0, 0, 0};
#pragma unroll
  for (int rr = 0; rr < 2; ++rr) {
    const int rho = 2 * w + rr;
    const char* pf = sm + (l15 & 3) * KC_STRIDE + 2 * (l15 & 12) + 16 * q + 64 * rho;
    const char* pg = sm + K1_U + 480 - 32 * l15 + 64 * rho + 16 * q;
    u32x4 Ff[8], Gf[8];
#pragma unroll
    for (int i = 0; i < 8; ++i) {
      Gf[i] = *(const u32x4*)(pg + 512 * (7 - i));
      const u32x2 a = *(const volatile LAS u32x2*)(pf + 512 * i), bb = *(const volatile LAS u32x2*)(pf + 512 * i + 8);
      Ff[i] = u32x4{a[0], a[1], bb[0], bb[1]};
    }
#pragma unroll
    for (int i = 0; i < 8; ++i)
#pragma unroll
      for (int e = 0; e + i < 8; ++e) acc[i + e] = mfma16(Ff[i], Gf[e], acc[i + e]);
  }
  ST(3);
  __syncthreads();
  f32x4 own[2];
  {
    char* p = sm + w * 1024 + lane * 16;
#define K1_PUT(W) do { _Pragma("unroll") for (int j = 0; j < 8; ++j) { if ((j >> 1) != (W)) *(f32x4*)(p + j * 4096) = acc[j]; } own[0] = acc[2 * (W)]; own[1] = acc[2 * (W) + 1]; } while (0)
    if (w == 0) K1_PUT(0); else if (w == 1) K1_PUT(1); else if (w == 2) K1_PUT(2); else K1_PUT(3);
  }
  __syncthreads();
  ST(4);
#pragma unroll
  for (int i = 0; i < 2; ++i) {
    const int j = 2 * w + i;
    f32x4 y = own[i];
#pragma unroll
    for (int s = 1; s < 4; ++s) y += *(const f32x4*)(sm + (j * 4 + ((w + s) & 3)) * 1024 + lane * 16);
    const int t = 256 * j + 16 * l15 + 4 * q;
    const u32x2 ur = *(const u32x2*)(sm + K1_U + 2 * (2044 - t));
    const float uv[4] = {bhi(ur[1]), blo(ur[1]), bhi(ur[0]), blo(ur[0])};
    const float xx[4] = {blo(xv[i][0]), bhi(xv[i][0]), blo(xv[i][1]), bhi(xv[i][1])};
    float h[4];
#pragma unroll
    for (int r = 0; r < 4; ++r) {
      const float ln = (xx[r] - bhi(st[i][r])) * __builtin_amdgcn_rcpf(blo(st[i][r]));
      h[r] = fmaf(ln, fmaf(uv[r], Dd, y[r]), xx[r]);
    }
    st64<P_HT>(rs, WS_HT + (d * S + t) * 2, u32x2{pk2(h[0], h[1]), pk2(h[2], h[3])});
  }
  ST(5); STAMP_OUT(1);
}

template <int N> DI void waitv() { asm volatile("s_waitcnt vmcnt(%0)" ::"n"(N) : "memory"); }
DI void dma16(u32x4 srd, u32 lds, u32 voff, u32 soff) {
  asm volatile("s_mov_b32 m0, %0\n\ts_nop 0\n\tbuffer_load_dwordx4 %1, %2, %3 offen lds" ::"s"(lds), "v"(voff), "s"(srd), "s"(soff) : "memory");
}
DI u32x4 mksrd(const void* p, u32 bytes) { const unsigned long long a = (unsigned long long)p; return u32x4{(u32)a, (u32)(a >> 32) & 0xffffu, bytes, 0x00020000u}; }
DI u32 ldsaddr(const char* p) { return (u32)(unsigned long long)(LAS const char*)p; }
DI void bar() { __builtin_amdgcn_s_barrier(); }

template <int CNT, int K> DI void tail_waits() { waitv<CNT * K>(); bar(); if constexpr (K > 0) tail_waits<CNT, K - 1>(); }
template <int CNT, int NS, int NST, int SLOT, int SPLIT = NS, int OFF0 = 0, int OFF1 = 0>
struct Loader {
  u32x4 srd; u32 lb; u32 voff[CNT], sstep[CNT], ldst[CNT]; int rot;
  static DI u32 slot_off(int j) { return (u32)(j * SLOT + (j < SPLIT ? OFF0 : OFF1)); }
  DI void issue(int st, int j) {
    int sr = st + rot; if (sr >= NST) sr -= NST;
    const u32 slot = slot_off(j);
#pragma unroll
    for (int i = 0; i < CNT; ++i) dma16(srd, lb + slot + ldst[i], voff[i], sr * sstep[i]);
  }
  template <int S0, int S1> DI void prologue() {
#pragma unroll
    for (int s = S0; s < S1; ++s) issue(s, s);
  }
  DI void run() {
    static_assert(CNT * (NS - 3) <= 63, "vmcnt");
    waitv<CNT * (NS - 3)>();
    bar();
    issue(NS - 2, NS - 2);
    int j = NS - 1;
    for (int n = 0; n + NS - 1 < NST; ++n) {
      waitv<CNT * (NS - 3)>();
      bar();
      issue(n + NS - 1, j);
      j = j == NS - 1 ? 0 : j + 1;
    }
    tail_waits<CNT, NS - 3>();
  }
};

constexpr int G1_NS = 6, G1_SLOT = 22 * 1024, G1_NST = 20;
#define G2NS 12
constexpr int G2_NS = G2NS, G2_SLOT = 10 * 1024, G2_RING = 2 * G1_SLOT, G2_NST = 27;
DI constexpr int g2_off(int j) { return j < 8 ? G2_RING + j * G2_SLOT : (j - 8) * G2_SLOT; }
#define G2HW 9
constexpr int G2_HW = G2HW;
constexpr int G_AUX = G1_NS * G1_SLOT, G_LDS = G_AUX + 11 * 1024;
static_assert(G2_RING + 8 * G2_SLOT <= G_AUX && (G2_NS - 8) * G2_SLOT <= G2_RING, "phase B ring lies in the slots that phase A has left");
typedef __attribute__((address_space(1))) u32 gu32;

template <int CNTA, int CNTB>
DI void mlp_loader(char* sm, char* ws, int rt, int c, int rt3, int dg, int rotA, int rotB, int wi, int lw, int lane) {
  const u32x4 srd = mksrd(ws, WS_END);
  const u32 lb = ldsaddr(sm);
  {
#pragma unroll
    for (int i = 0; i < 3; ++i) {
      const int p = lw + 4 * i;
      if (p < 10) dma16(srd, lb + G_AUX + p * 1024, WS_HT + (80 * dg + 8 * p + (lane >> 3)) * (S * 2) + rt3 * 128 + (lane & 7) * 16, 0);
    }
    Loader<CNTA, G1_NS, G1_NST, G1_SLOT> L;
    L.srd = srd; L.lb = lb; L.rot = rotA;
    const int l15 = lane & 15, q = lane >> 4;
#pragma unroll
    for (int i = 0; i < CNTA; ++i) {
      const int p = lw + 4 * i;
      L.ldst[i] = p * 1024;
      if (p < 14) { L.voff[i] = WS_W1T + (c * 20 * 14 + p) * 1024 + lane * 16; L.sstep[i] = 14 * 1024; }
      else { const int kk = 4 * (p - 14) + q, f = (kk & 3) | (((kk >> 3) & 1) << 2);
             L.voff[i] = WS_HT + kk * (S * 2) + rt * 256 + 16 * (l15 ^ (f << 1)); L.sstep[i] = 32 * S * 2; }
    }
    L.template prologue<0, G1_NS - 2>();
    L.run();
  }
  Loader<CNTB, G2_NS, G2_NST, G2_SLOT, 8, G2_RING, -8 * G2_SLOT> L;
  L.srd = srd; L.lb = lb; L.rot = rotB;
#pragma unroll
  for (int i = 0; i < CNTB; ++i) { const int p = lw + 4 * i; L.ldst[i] = p * 1024; L.voff[i] = WS_W2T + (dg * 54 * 5 + p) * 1024 + lane * 16; L.sstep[i] = 10 * 1024;
  }
  L.template prologue<0, (G2_NS - 2 < 8 ? G2_NS - 2 : 8)>();
  bar();
  L.template prologue<8, G2_NS - 2>();
  L.run();
  __syncthreads();
  __syncthreads();
}

template <int NT, int T0, bool STAT>
DI void g1_body(char* sm, char* ws, int rt, int c, int w, int lane) {
  const int tw = w & 3, l15 = lane & 15, q = lane >> 4;
  const int qp = l15 >> 2, p4 = lane & 3, msk = 2 * qp | ((q & 1) << 3);
  int hoff[2];
#pragma unroll
  for (int i = 0; i < 2; ++i) hoff[i] = 14336 + (8 * q + qp) * 256 + 16 * ((4 * tw + 2 * i + (p4 >> 1)) ^ msk) + 8 * (p4 & 1);
  const int woff = T0 * 1024 + lane * 16;
  u32x4 W[NT];
  u32x2 h0a, h0b, h1a, h1b;
  f32x4 acc[2][NT], accS[2], accG[2];
#pragma unroll
  for (int i = 0; i < 2; ++i) {
    accS[i] = f32x4{0, 0, 0, 0}; accG[i] = f32x4{0, 0, 0, 0};
#pragma unroll
    for (int m = 0; m < NT; ++m) acc[i][m] = f32x4{0, 0, 0, 0};
  }
  const u32x4 ones = {0x3f803f80u, 0x3f803f80u, 0x3f803f80u, 0x3f803f80u};
  auto half0 = [&](const char* slot) {
    const u32x4 h0 = {h0a[0], h0a[1], h0b[0], h0b[1]};
#pragma unroll
    for (int m = 0; m < NT; ++m) {
      acc[0][m] = mfma16(W[m], h0, acc[0][m]);
      if (m == 0) h1a = trd(slot + hoff[1]);
      if (m == 1) h1b = trd(slot + hoff[1] + 1024);
      pin();
    }
    if (STAT) { accS[0] = mfma16(ones, h0, accS[0]); accG[0] = mfma16(h0, h0, accG[0]); pin(); }
  };
  auto half1 = [&](const char* nslot, bool doread) {
    const u32x4 h1 = {h1a[0], h1a[1], h1b[0], h1b[1]};
#pragma unroll
    for (int m = 0; m < NT; ++m) {
      acc[1][m] = mfma16(W[m], h1, acc[1][m]);
      if (doread) {
        W[m] = *(const u32x4*)(nslot + woff + m * 1024);
        if (m == 0) h0a = trd(nslot + hoff[0]);
        if (m == 1) h0b = trd(nslot + hoff[0] + 1024);
      }
      pin();
    }
    if (STAT) { accS[1] = mfma16(ones, h1, accS[1]); accG[1] = mfma16(h1, h1, accG[1]); pin(); }
  };
  bar();
#pragma unroll
  for (int m = 0; m < NT; ++m) {
    W[m] = *(const u32x4*)(sm + woff + m * 1024);
    if (m == 0) h0a = trd(sm + hoff[0]);
    if (m == 1) h0b = trd(sm + hoff[0] + 1024);
  }
  u32 cur = 0;
  half0(sm);
  for (int n = 0; n < G1_NST - 1; ++n) {
    bar();
    cur = cur == (G1_NS - 1) * G1_SLOT ? 0u : cur + G1_SLOT;
    half1(sm + cur, true);
    half0(sm + cur);
  }
  half1(sm, false);
  const rsrc_t rs = mkrsrc(ws, WS_END);
  const int ksb = 3 * c;
#pragma unroll
  for (int i = 0; i < 2; ++i) {
    const u32 ttile = 8 * rt + 2 * tw + i;
    u32 o[NT / 2][2];
#pragma unroll
    for (int pr = 0; pr < NT / 2; ++pr) {
      const f32x4 g = acc[i][2 * pr], u = acc[i][2 * pr + 1];
      o[pr][0] = pk2(fmaxf(g[0], 0.f) * u[0], fmaxf(g[1], 0.f) * u[1]); o[pr][1] = pk2(fmaxf(g[2], 0.f) * u[2], fmaxf(g[3], 0.f) * u[3]);
    }
    if (NT == 8) {
      st128(rs, WS_HM + ((ttile * 54 + ksb) * 64 + lane) * 16, u32x4{o[0][0], o[0][1], o[1][0], o[1][1]});
      st128(rs, WS_HM + ((ttile * 54 + ksb + 1) * 64 + lane) * 16, u32x4{o[2 % (NT / 2)][0], o[2 % (NT / 2)][1], o[3 % (NT / 2)][0], o[3 % (NT / 2)][1]});
    } else {
      st128(rs, WS_HM + ((ttile * 54 + ksb + 2) * 64 + lane) * 16, u32x4{o[0][0], o[0][1], o[1][0], o[1][1]});
      if (NT == 6) st64(rs, WS_HM + ((ttile * 54 + 48 + (c >> 1)) * 64 + lane) * 16 + 8 * (c & 1), u32x2{o[2 % (NT / 2)][0], o[2 % (NT / 2)][1]});
    }
    if (STAT) {
      if (tw == c - 12 && q == (l15 >> 2)) {
        const int r = l15 & 3;
        const float s2 = r == 0 ? accG[i][0] : r == 1 ? accG[i][1] : r == 2 ? accG[i][2] : accG[i][3];
        const float mean = accS[i][0] * (1.f / D), var = fmaxf(s2 * (1.f / D) - mean * mean, 0.f);
        const float rinv = __builtin_amdgcn_rcpf(rbf(__builtin_amdgcn_sqrtf(rbf(rbf(var) + EPS))));
        __builtin_amdgcn_raw_buffer_store_b32(__float_as_uint(rinv * rinv), rs, WS_R2 + (16 * ttile + l15) * 4, 0, AUX_WT);
      }
    }
  }
}

DI void g2_body(char* sm, char* ws, u16* out, int rt3, int dg, int rot, int w, int lane STAMP_PARAM) {
  const int tt = w & 3, kh = w >> 2, l15 = lane & 15, q = lane >> 4;
  const rsrc_t rs = mkrsrc(ws, WS_END);
  const u32 hbase = WS_HM + ((4 * rt3 + tt) * 54 + kh) * 1024 + lane * 16;
  u32x4 H[G2_HW];
  auto hload = [&](int n) { int sr = n + rot; if (sr >= G2_NST) sr -= G2_NST;
    return __builtin_amdgcn_raw_buffer_load_b128(rs, hbase, sr * 2048, AUX_WT);
  };
#pragma unroll
  for (int j = 0; j < G2_HW; ++j) H[j] = hload(j);
  const float r2 = __uint_as_float(__builtin_amdgcn_raw_buffer_load_b32(rs, WS_R2 + (64 * rt3 + 16 * tt + l15) * 4, 0, AUX_WT));
  pin();
  const char* wb = sm + kh * 5 * 1024 + lane * 16;
  u32x4 Wa[5], Wb[5];
  f32x4 acc[5];
#pragma unroll
  for (int m = 0; m < 5; ++m) { acc[m] = f32x4{0, 0, 0, 0}; Wa[m] = *(const u32x4*)(wb + g2_off(0) + m * 1024); }
  auto step = [&](u32x4 (&Wc)[5], u32x4 (&Wn)[5], int n) {
    if (n + 1 < G2_NST) bar();
#pragma unroll
    for (int m = 0; m < 5; ++m) {
      if (n + 1 < G2_NST) Wn[m] = *(const u32x4*)(wb + g2_off((n + 1) % G2_NS) + m * 1024);
      acc[m] = mfma16(Wc[m], H[n % G2_HW], acc[m]);
      pin();
    }
    if (n + G2_HW < G2_NST) { H[n % G2_HW] = hload(n + G2_HW); pin(); }
  };
#pragma unroll
  for (int n = 0; n < G2_NST; ++n) { if (n & 1) step(Wb, Wa, n); else step(Wa, Wb, n);
  }
  ST(4);
  __syncthreads();
  if (kh == 1) {
#pragma unroll
    for (int m = 0; m < 5; ++m) *(f32x4*)(sm + (tt * 5 + m) * 1024 + lane * 16) = acc[m];
  }
  __syncthreads();
  if (kh == 0) {
    const u16* res = (const u16*)(sm + G_AUX);
#pragma unroll
    for (int m = 0; m < 5; ++m) {
      const f32x4 o = acc[m] + *(const f32x4*)(sm + (tt * 5 + m) * 1024 + lane * 16);
      float r[4];
#pragma unroll
      for (int j = 0; j < 4; ++j) r[j] = __uint_as_float((u32)res[(16 * m + 4 * q + j) * 64 + 16 * tt + l15] << 16) + rbf(o[j] * r2);
      *(u32x2*)(out + (64 * rt3 + 16 * tt + l15) * D + 80 * dg + 16 * m + 4 * q) = u32x2{pk2(r[0], r[1]), pk2(r[2], r[3])};
    }
  }
}

DI void mlp_warm(char* sm, char* ws, int dg, int i, int lane) {
  const u32x4 srd = mksrd(ws, WS_END);
  const u32 lb = ldsaddr(sm) + G_AUX + 10 * 1024;
  const u32 voff = WS_W2T + (dg >> 2) * (4 * 54 * 5 * 1024) + i * 34560 + lane * 16;
  bar();
  for (int n = 0; n < 17; ++n) { bar(); dma16(srd, lb, voff, n * 2048); dma16(srd, lb, voff, n * 2048 + 1024); }
  waitv<0>();
}
constexpr int MLP_THREADS = 768;
extern "C" __global__ __launch_bounds__(MLP_THREADS) void k2_mlp(char* __restrict__ ws, u16* __restrict__ out) {
  extern __shared__ __attribute__((aligned(16))) char sm[];
  const int b = blockIdx.x, x = b & 7, i = b >> 3, cq = x >> 1;
  const int rt = 8 * (x & 1) + (i & 7), c = 4 * cq + (i >> 3);
  const int rt3 = 2 * rt + (cq >> 1), dg = 4 * (cq & 1) + (c & 3);
  const int lane = threadIdx.x & 63, w = __builtin_amdgcn_readfirstlane(threadIdx.x >> 6);
#define ROTA 0
#define ROTB 0
  const int rotA = ROTA ? (5 * (c & 3) + (5 * (i & 7)) / 2) % G1_NST : 0, rotB = ROTB ? (7 * (dg & 3) + (27 * (i & 7)) / 8) % G2_NST : 0;
  if (w == 12) { mlp_warm(sm, ws, dg, i, lane); return; }
  if (w >= 8) {
    if (w < 10) mlp_loader<6, 3>(sm, ws, rt, c, rt3, dg, rotA, rotB, i, w - 8, lane); else mlp_loader<5, 2>(sm, ws, rt, c, rt3, dg, rotA, rotB, i, w - 8, lane);
    return;
  }
  STAMP_DECL ST(0);
  if (w < 4) g1_body<8, 0, false>(sm, ws, rt, c, w, lane);
  else if (c < 12) g1_body<6, 8, false>(sm, ws, rt, c, w, lane);
  else g1_body<4, 8, true>(sm, ws, rt, c, w, lane);
  ST(1);
  asm volatile("s_waitcnt vmcnt(0)" ::: "memory");
  bar();
  ST(2);
  if (threadIdx.x == 0) {
    gu32* cnt = (gu32*)(ws + WS_CNT) + (rt3 >> 1) * 32;
    __hip_atomic_fetch_add((gu32*)(ws + WS_CNT) + rt * 32, 1u, __ATOMIC_RELAXED, __HIP_MEMORY_SCOPE_AGENT);
    for (u32 spins = 0; __hip_atomic_load(cnt, __ATOMIC_RELAXED, __HIP_MEMORY_SCOPE_AGENT) < 16u && spins < (1u << 20); ++spins) __builtin_amdgcn_s_sleep(1);
  }
  __builtin_amdgcn_fence(__ATOMIC_ACQUIRE, "wavefront");
  bar();
  ST(3);
  g2_body(sm, ws, out, rt3, dg, rotB, w, lane STAMP_PASS);
  ST(5); STAMP_OUT(2);
}

extern "C" __global__ void k9_end() {}

extern "C" void kernel_launch(void* const* d_in, const int* in_sizes, int n_in, void* d_out, int out_size, void* d_ws, size_t ws_size, hipStream_t stream) {
  const u16* q = (const u16*)d_in[0]; const u16* k = (const u16*)d_in[1]; const u16* v = (const u16*)d_in[2];
  const u16* shk = (const u16*)d_in[3]; const u16* ssmk = (const u16*)d_in[4]; const u16* ssmD = (const u16*)d_in[5];
  const u16* wg = (const u16*)d_in[7]; const u16* wu = (const u16*)d_in[8]; const u16* wd = (const u16*)d_in[9];
  char* ws = (char*)d_ws;
  (void)hipFuncSetAttribute((const void*)k2_mlp, hipFuncAttributeMaxDynamicSharedMemorySize, G_LDS);
  k0_prep<<<448, 256, 0, stream>>>(q, k, v, shk, ws);
  k1_conv<<<D + NW1 + NW2, 256, K1_LDS, stream>>>(ssmk, ssmD, wg, wu, wd, ws);
  k2_mlp<<<256, MLP_THREADS, G_LDS, stream>>>(ws, (u16*)d_out);
}
```

```cpp
#include <hip/hip_runtime.h>
#include <stdint.h>
#define NO_WARM 1

typedef unsigned short u16;
typedef unsigned int u32;
typedef u32 u32x2 __attribute__((ext_vector_type(2)));
typedef u32 u32x4 __attribute__((ext_vector_type(4)));
typedef short s16x4 __attribute__((ext_vector_type(4)));
typedef short s16x8 __attribute__((ext_vector_type(8)));
typedef float f32x2 __attribute__((ext_vector_type(2)));
typedef float f32x4 __attribute__((ext_vector_type(4)));
typedef __bf16 bf16x2 __attribute__((ext_vector_type(2)));
#define LAS __attribute__((address_space(3)))
#define DI __device__ __forceinline__

constexpr int S = 2048, D = 640, F = 1728;
constexpr float EPS = 1e-5f;

constexpr u32 WS_URT = 0;
constexpr u32 WS_XT  = WS_URT + D * S * 2;
constexpr u32 WS_HT  = WS_XT + D * S * 2;
constexpr u32 WS_ST1 = WS_HT + D * S * 2;
constexpr u32 WS_R2  = WS_ST1 + S * 4;
constexpr u32 WS_CNT = WS_R2 + S * 4;
constexpr u32 WS_W1T = WS_CNT + 16384;
constexpr u32 WS_W2T = WS_W1T + 16 * 20 * 14 * 1024;
constexpr u32 WS_HM  = WS_W2T + 8 * 54 * 5 * 1024;
constexpr u32 WS_END = WS_HM + 128 * 54 * 1024;

typedef __amdgpu_buffer_rsrc_t rsrc_t;
DI float blo(u32 u) { return __uint_as_float(u << 16); }
DI float bhi(u32 u) { return __uint_as_float(u & 0xffff0000u); }
DI u32 pk2(float a, float b) { bf16x2 r; r[0] = (__bf16)a; r[1] = (__bf16)b; return __builtin_bit_cast(u32, r); }
DI float rbf(float a) { return blo(pk2(a, a)); }
DI f32x4 mfma16(u32x4 a, u32x4 b, f32x4 c) {
  return __builtin_amdgcn_mfma_f32_16x16x32_bf16(__builtin_bit_cast(s16x8, a), __builtin_bit_cast(s16x8, b), c, 0, 0, 0);
}
DI rsrc_t mkrsrc(const void* p, u32 bytes) { return __builtin_amdgcn_make_buffer_rsrc((void*)p, 0, bytes, 0x00020000); }
constexpr int AUX_WT = 16, AUX_NT = 2;
DI u32x4 ld128nt(rsrc_t rs, u32 off) { return __builtin_amdgcn_raw_buffer_load_b128(rs, off, 0, AUX_NT); }
DI u32x4 ld128(rsrc_t rs, u32 off) { return __builtin_amdgcn_raw_buffer_load_b128(rs, off, 0, 0); }
DI u32x2 ld64(rsrc_t rs, u32 off) { return __builtin_amdgcn_raw_buffer_load_b64(rs, off, 0, 0); }
template <int AUX = AUX_WT> DI void st128(rsrc_t rs, u32 off, u32x4 v) { __builtin_amdgcn_raw_buffer_store_b128(v, rs, off, 0, AUX); }
template <int AUX = AUX_WT> DI void st64(rsrc_t rs, u32 off, u32x2 v) { __builtin_amdgcn_raw_buffer_store_b64(v, rs, off, 0, AUX); }
constexpr int P_HT = 0, P_K0 = 0, P_W = 16;
DI void pin() { __builtin_amdgcn_sched_barrier(0); }
DI u32x2 trd(const char* p) { return __builtin_bit_cast(u32x2, __builtin_amdgcn_ds_read_tr16_b64_v4i16((LAS s16x4*)p)); }
DI u32x4 tr_frag(const char* p0, const char* p1) { const u32x2 a = trd(p0), b = trd(p1); return u32x4{a[0], a[1], b[0], b[1]}; }

#define STAMP_DECL
#define STAMP_PARAM
#define STAMP_PASS
#define ST(i) do {} while (0)
#define STAMP_OUT(K) do {} while (0)

extern "C" __global__ __launch_bounds__(256) void k0_prep(const u16* __restrict__ x, const u16* __restrict__ k, const u16* __restrict__ v, const u16* __restrict__ shk, char* __restrict__ ws) {
  __shared__ __attribute__((aligned(16))) u32 sm[2 * 64 * 32];
  const int b = blockIdx.x, tid = threadIdx.x;
  STAMP_DECL ST(0);
  const rsrc_t rx = mkrsrc(x, S * D * 2);
  if (b == 447 && tid < 16) ((u32*)(ws + WS_CNT))[tid * 32] = 0u;
  if (b == 446) {
    unsigned long long c0, c1;
    asm volatile("s_getpc_b64 s[40:41]\n\ts_add_u32 s40, s40, k1_conv@rel32@lo+4\n\ts_addc_u32 s41, s41, k1_conv@rel32@hi+12\n\ts_mov_b64 %0, s[40:41]\n\t"
                 "s_getpc_b64 s[40:41]\n\ts_add_u32 s40, s40, k9_end@rel32@lo+4\n\ts_addc_u32 s41, s41, k9_end@rel32@hi+12\n\ts_mov_b64 %1, s[40:41]"
                 : "=s"(c0), "=s"(c1) :: "s40", "s41", "scc");
    const rsrc_t rc = mkrsrc((const void*)c0, (u32)(c1 - c0));
    const u32 p0 = __builtin_amdgcn_raw_buffer_load_b32(rc, tid * 128, 0, 0), p1 = __builtin_amdgcn_raw_buffer_load_b32(rc, (tid + 256) * 128, 0, 0);
    unsigned long long kd;
    asm volatile("s_getpc_b64 s[40:41]\n\ts_add_u32 s40, s40, k0_prep.kd@rel32@lo+4\n\ts_addc_u32 s41, s41, k0_prep.kd@rel32@hi+12\n\ts_mov_b64 %0, s[40:41]" : "=s"(kd) :: "s40", "s41", "scc");
    const u32 p2 = __builtin_amdgcn_raw_buffer_load_b32(mkrsrc((const void*)(kd & ~127ull), 512), (tid & 3) * 128, 0, 0);
    const unsigned long long ka = (unsigned long long)__builtin_amdgcn_kernarg_segment_ptr() & ~4095ull;
    const u32 p3 = __builtin_amdgcn_raw_buffer_load_b32(mkrsrc((const void*)ka, 4096), (tid & 31) * 128, 0, 0);
    asm volatile("" :: "v"(p0), "v"(p1), "v"(p2), "v"(p3));
  }
  if (b < 320) {
    const rsrc_t rk = mkrsrc(k, S * D * 2), rv = mkrsrc(v, S * D * 2), rw = mkrsrc(shk, D * 8);
    const int tb = (b & 7) + 8 * ((b >> 3) / 10), cb = (b >> 3) % 10, c = tid & 7, rg = tid >> 3;
    const int t = 64 * tb + 2 * rg, ch0 = 64 * cb + 8 * c;
    const u32 o = (u32)(t * D + ch0) * 2u;
    u32x4 kr[5], vr[2], xr[2], w[4];
#pragma unroll
    for (int i = 0; i < 5; ++i) kr[i] = ld128nt(rk, o + (u32)((i - 3) * D * 2));
#pragma unroll
    for (int i = 0; i < 2; ++i) { vr[i] = ld128nt(rv, o + i * D * 2); xr[i] = ld128nt(rx, o + i * D * 2); }
#pragma unroll
    for (int i = 0; i < 4; ++i) w[i] = ld128(rw, ch0 * 8 + 16 * i);
    pin();
    u32* sU = sm; u32* sX = sm + 64 * 32;
    const int colU = 31 - rg, colX = rg;
    const int pU = 4 * ((colU >> 2) ^ c) + (colU & 3), pX = 4 * ((colX >> 2) ^ c) + (colX & 3);
#pragma unroll
    for (int j = 0; j < 8; ++j) {
      float kk[5];
#pragma unroll
      for (int i = 0; i < 5; ++i) kk[i] = (j & 1) ? bhi(kr[i][j >> 1]) : blo(kr[i][j >> 1]);
      const u32 wa = w[j >> 1][(j & 1) * 2], wb = w[j >> 1][(j & 1) * 2 + 1];
      const float w0 = blo(wa), w1 = bhi(wa), w2 = blo(wb), w3 = bhi(wb);
      const float s0 = kk[0] * w3 + kk[1] * w2 + kk[2] * w1 + kk[3] * w0;
      const float s1 = kk[1] * w3 + kk[2] * w2 + kk[3] * w1 + kk[4] * w0;
      const u32 sp = pk2(s0, s1);
      const float v0 = (j & 1) ? bhi(vr[0][j >> 1]) : blo(vr[0][j >> 1]);
      const float v1 = (j & 1) ? bhi(vr[1][j >> 1]) : blo(vr[1][j >> 1]);
      sU[(8 * c + j) * 32 + pU] = pk2(bhi(sp) * v1, blo(sp) * v0);
      sX[(8 * c + j) * 32 + pX] = (j & 1) ? __builtin_amdgcn_perm(xr[1][j >> 1], xr[0][j >> 1], 0x07060302u) : __builtin_amdgcn_perm(xr[1][j >> 1], xr[0][j >> 1], 0x05040100u);
    }
    ST(1);
    __syncthreads();
#pragma unroll
    for (int i = 0; i < 2; ++i) {
      const int piece = tid + 256 * i, ch = piece >> 3, g = piece & 7, p = ch * 32 + 4 * (g ^ (ch >> 3));
      st128<P_K0>(mkrsrc(ws, WS_END), WS_URT + ((64 * cb + ch) * S + (1984 - 64 * tb) + 8 * g) * 2, *(const u32x4*)(sU + p));
      st128<P_K0>(mkrsrc(ws, WS_END), WS_XT + ((64 * cb + ch) * S + 64 * tb + 8 * g) * 2, *(const u32x4*)(sX + p));
    }
  } else {
    const int sb = b - 320, row = 64 * ((sb & 7) + 8 * (sb >> 5)) + 16 * ((sb >> 3) & 3) + (tid >> 4), s = tid & 15;
    u32x4 r[5];
#pragma unroll
    for (int i = 0; i < 5; ++i) r[i] = ld128nt(rx, (row * D + 8 * (s + 16 * i)) * 2);
    pin();
    float sum = 0.f;
#pragma unroll
    for (int i = 0; i < 5; ++i)
#pragma unroll
      for (int j = 0; j < 4; ++j) sum += blo(r[i][j]) + bhi(r[i][j]);
#pragma unroll
    for (int o = 1; o < 16; o <<= 1) sum += __shfl_xor(sum, o);
    const float mean = sum * (1.f / D);
    float q = 0.f;
#pragma unroll
    for (int i = 0; i < 5; ++i)
#pragma unroll
      for (int j = 0; j < 4; ++j) { const float a = blo(r[i][j]) - mean, c2 = bhi(r[i][j]) - mean; q += a * a + c2 * c2; }
#pragma unroll
    for (int o = 1; o < 16; o <<= 1) q += __shfl_xor(q, o);
    const float sd = __builtin_amdgcn_sqrtf(rbf(rbf(q * (1.f / D)) + EPS));
    if (s == 0) ((u32*)(ws + WS_ST1))[row] = pk2(sd, mean);
  }
  ST(2); STAMP_OUT(0);
}

constexpr int NW1 = 112;
constexpr int NTASK2 = 540, TPW = 2, NW2 = (NTASK2 + 4 * TPW - 1) / (4 * TPW);
constexpr int KC_STRIDE = 4160;
constexpr int K1_U = 32 * 1024, K1_LDS = K1_U + 2304 * 2;

DI void w1_strip(int r, const u16* wg, const u16* wu, char* ws, char* sm) {
  const int p = 8 * (r >> 4) + (r & 7), s = 2 * p + ((r >> 3) & 1);
  if (s >= 108) return;
  const int tid = threadIdx.x, lane = tid & 63, w = __builtin_amdgcn_readfirstlane(tid >> 6);
  const int m = s / 54, col0 = 32 * (s % 54), c4 = tid & 3, kg = tid >> 2;
  const rsrc_t rw = mkrsrc(m ? wu : wg, D * F * 2), rs = mkrsrc(ws, WS_END);
  u32x4 rr[10];
#pragma unroll
  for (int i = 0; i < 10; ++i) rr[i] = ld128nt(rw, ((kg + 64 * i) * F + col0 + 8 * c4) * 2);
  pin();
  const int g = lane >> 4, qp = (lane & 15) >> 2, p4 = lane & 3, c2 = w & 1;
  const u32x4 ones = {0x3f803f80u, 0x3f803f80u, 0x3f803f80u, 0x3f803f80u};
  u32x4 fr[10];
  f32x4 cs = {0, 0, 0, 0};
#pragma unroll
  for (int hf = 0; hf < 2; ++hf) {
    if (hf) __syncthreads();
#pragma unroll
    for (int i = 0; i < 5; ++i) *(u32x4*)(sm + (kg + 64 * i) * 80 + 16 * c4) = rr[5 * hf + i];
    __syncthreads();
#pragma unroll
    for (int i = 0; i < 5; ++i) {
      const char* im = sm + (32 * ((w >> 1) + 2 * i) + 8 * g + qp) * 80 + 32 * c2 + 8 * p4;
      fr[5 * hf + i] = tr_frag(im, im + 4 * 80);
      cs = mfma16(ones, fr[5 * hf + i], cs);
    }
  }
  float* red = (float*)(sm + 320 * 80);
  if (lane < 16) red[w * 16 + lane] = cs[0];
  __syncthreads();
  const float mu = (red[w * 16 + (lane & 15)] + red[(w ^ 2) * 16 + (lane & 15)]) * (1.f / D);
  const int ft = (col0 >> 4) + c2;
  const int c = ft < 84 ? ft / 7 : 12 + (ft - 84) / 6, pair = ft < 84 ? ft % 7 : (ft - 84) % 6;
#pragma unroll
  for (int i = 0; i < 10; ++i) {
    const int ks = 10 * (i / 5) + (w >> 1) + 2 * (i % 5);
    const u32x4 a = fr[i];
    st128<P_W>(rs, WS_W1T + ((c * 20 + ks) * 14 + 2 * pair + m) * 1024 + lane * 16,
          u32x4{pk2(blo(a[0]) - mu, bhi(a[0]) - mu), pk2(blo(a[1]) - mu, bhi(a[1]) - mu), pk2(blo(a[2]) - mu, bhi(a[2]) - mu), pk2(blo(a[3]) - mu, bhi(a[3]) - mu)});
  }
}
DI void w2_tasks(int pb, const u16* wd, char* ws, char* sm) {
  const int tid = threadIdx.x, lane = tid & 63, w = __builtin_amdgcn_readfirstlane(tid >> 6);
  const rsrc_t rw = mkrsrc(wd, D * F * 2), rs = mkrsrc(ws, WS_END);
  char* im = sm + w * (32 * 144);
  u32x4 ld[TPW][4];
  int task[TPW];
#pragma unroll
  for (int u = 0; u < TPW; ++u) {
    int t = (pb * 4 + w) * TPW + u; if (t >= NTASK2) t = NTASK2 - 1;
    task[u] = t;
    const int ks = t / 10, dr = t % 10;
    int lo, hi;
    if (ks < 36) { lo = 7 * (ks / 3) + 2 * (ks % 3); hi = lo + 1; } else if (ks < 48) { lo = 84 + 2 * (ks - 36); hi = lo + 1; } else { lo = 14 * (ks - 48) + 6; hi = lo + 7; }
#pragma unroll
    for (int i = 0; i < 4; ++i) { const int r = (lane >> 3) + 8 * i; ld[u][i] = ld128nt(rw, ((16 * (r < 16 ? lo : hi) + (r & 15)) * D + 64 * dr + 8 * (lane & 7)) * 2); }
  }
  pin();
  const int g = lane >> 4, qp = (lane & 15) >> 2, p4 = lane & 3;
#pragma unroll
  for (int u = 0; u < TPW; ++u) {
#pragma unroll
    for (int i = 0; i < 4; ++i) *(u32x4*)(im + ((lane >> 3) + 8 * i) * 144 + 16 * (lane & 7)) = ld[u][i];
    const int ks = task[u] / 10;
#pragma unroll
    for (int c4 = 0; c4 < 4; ++c4) {
      const int dt = 4 * (task[u] % 10) + c4;
      const u32x4 fr = tr_frag(im + (4 * g + qp) * 144 + 32 * c4 + 8 * p4, im + (16 + 4 * g + qp) * 144 + 32 * c4 + 8 * p4);
      st128<P_W>(rs, WS_W2T + (((dt / 5) * 54 + ks) * 5 + dt % 5) * 1024 + lane * 16, fr);
    }
  }
}

extern "C" __global__ __launch_bounds__(256, 4) void k1_conv(const u16* __restrict__ ssmk, const u16* __restrict__ ssmD, const u16* __restrict__ wg, const u16* __restrict__ wu, const u16* __restrict__ wd, char* __restrict__ ws) {
  extern __shared__ __attribute__((aligned(16))) char sm[];
  const int b = blockIdx.x, tid = threadIdx.x;
  STAMP_DECL ST(0);
  if (b >= D + NW1) { w2_tasks(b - D - NW1, wd, ws, sm); ST(5); STAMP_OUT(1); return; }
  if (b >= D) { w1_strip(b - D, wg, wu, ws, sm); ST(5); STAMP_OUT(1); return; }
  const int d = b, lane = tid & 63, w = __builtin_amdgcn_readfirstlane(tid >> 6);
  const int l15 = lane & 15, q = lane >> 4;
  const rsrc_t rk = mkrsrc(ssmk + d * S, S * 2), rs = mkrsrc(ws, WS_END);
  const u32x4 c0 = ld128nt(rk, 16 * tid), c1 = ld128nt(rk, 16 * tid + 16);
  const u32x4 uu = ld128(rs, WS_URT + d * (S * 2) + 16 * tid);
  u32x2 xv[2]; u32x4 st[2];
#pragma unroll
  for (int i = 0; i < 2; ++i) {
    const int t = 256 * (2 * w + i) + 16 * l15 + 4 * q;
    xv[i] = ld64(rs, WS_XT + (d * S + t) * 2);
    st[i] = ld128(rs, WS_ST1 + t * 4);
  }
  const float Dd = blo(ssmD[d]);
  pin();
  ST(1);
  auto build = [&](int m, const u32x4& a, const u32x4& bb) {
    const u32 ww[8] = {a[0], a[1], a[2], a[3], bb[0], bb[1], bb[2], bb[3]};
    u32x4 o0, o1, o2, o3;
#pragma unroll
    for (int i = 0; i < 4; ++i) {
      o0[i] = __builtin_amdgcn_alignbyte(ww[i + 1], ww[i], 2); o1[i] = ww[i + 1];
      o2[i] = __builtin_amdgcn_alignbyte(ww[i + 2], ww[i + 1], 2); o3[i] = ww[i + 2];
    }
    *(u32x4*)(sm + 0 * KC_STRIDE + 16 * m) = o0; *(u32x4*)(sm + 1 * KC_STRIDE + 16 * m) = o1;
    *(u32x4*)(sm + 2 * KC_STRIDE + 16 * m) = o2; *(u32x4*)(sm + 3 * KC_STRIDE + 16 * m) = o3;
  };
  const u32x4 zero4 = {0, 0, 0, 0};
  build(tid + 2, c0, c1);
  if (tid < 2) build(1 - tid, zero4, tid == 0 ? c0 : zero4);
  *(u32x4*)(sm + K1_U + 16 * tid) = uu;
  if (tid < 32) *(u32x4*)(sm + K1_U + 4096 + 16 * tid) = zero4;
  __syncthreads();
  ST(2);
  f32x4 acc[8];
#pragma unroll
  for (int j = 0; j < 8; ++j) acc[j] = f32x4{0, 0, 0, 0};
#pragma unroll
  for (int rr = 0; rr < 2; ++rr) {
    const int rho = 2 * w + rr;
    const char* pf = sm + (l15 & 3) * KC_STRIDE + 2 * (l15 & 12) + 16 * q + 64 * rho;
    const char* pg = sm + K1_U + 480 - 32 * l15 + 64 * rho + 16 * q;
    u32x4 Ff[8], Gf[8];
#pragma unroll
    for (int i = 0; i < 8; ++i) {
      Gf[i] = *(const u32x4*)(pg + 512 * (7 - i));
      const u32x2 a = *(const volatile LAS u32x2*)(pf + 512 * i), bb = *(const volatile LAS u32x2*)(pf + 512 * i + 8);
      Ff[i] = u32x4{a[0], a[1], bb[0], bb[1]};
    }
#pragma unroll
    for (int i = 0; i < 8; ++i)
#pragma unroll
      for (int e = 0; e + i < 8; ++e) acc[i + e] = mfma16(Ff[i], Gf[e], acc[i + e]);
  }
  ST(3);
  __syncthreads();
  f32x4 own[2];
  {
    char* p = sm + w * 1024 + lane * 16;
#define K1_PUT(W) do { _Pragma("unroll") for (int j = 0; j < 8; ++j) { if ((j >> 1) != (W)) *(f32x4*)(p + j * 4096) = acc[j]; } own[0] = acc[2 * (W)]; own[1] = acc[2 * (W) + 1]; } while (0)
    if (w == 0) K1_PUT(0); else if (w == 1) K1_PUT(1); else if (w == 2) K1_PUT(2); else K1_PUT(3);
  }
  __syncthreads();
  ST(4);
#pragma unroll
  for (int i = 0; i < 2; ++i) {
    const int j = 2 * w + i;
    f32x4 y = own[i];
#pragma unroll
    for (int s = 1; s < 4; ++s) y += *(const f32x4*)(sm + (j * 4 + ((w + s) & 3)) * 1024 + lane * 16);
    const int t = 256 * j + 16 * l15 + 4 * q;
    const u32x2 ur = *(const u32x2*)(sm + K1_U + 2 * (2044 - t));
    const float uv[4] = {bhi(ur[1]), blo(ur[1]), bhi(ur[0]), blo(ur[0])};
    const float xx[4] = {blo(xv[i][0]), bhi(xv[i][0]), blo(xv[i][1]), bhi(xv[i][1])};
    float h[4];
#pragma unroll
    for (int r = 0; r < 4; ++r) {
      const float ln = (xx[r] - bhi(st[i][r])) * __builtin_amdgcn_rcpf(blo(st[i][r]));
      h[r] = fmaf(ln, fmaf(uv[r], Dd, y[r]), xx[r]);
    }
    st64<P_HT>(rs, WS_HT + (d * S + t) * 2, u32x2{pk2(h[0], h[1]), pk2(h[2], h[3])});
  }
  ST(5); STAMP_OUT(1);
}

template <int N> DI void waitv() { asm volatile("s_waitcnt vmcnt(%0)" ::"n"(N) : "memory"); }
DI void dma16(u32x4 srd, u32 lds, u32 voff, u32 soff) {
  asm volatile("s_mov_b32 m0, %0\n\ts_nop 0\n\tbuffer_load_dwordx4 %1, %2, %3 offen lds" ::"s"(lds), "v"(voff), "s"(srd), "s"(soff) : "memory");
}
DI u32x4 mksrd(const void* p, u32 bytes) { const unsigned long long a = (unsigned long long)p; return u32x4{(u32)a, (u32)(a >> 32) & 0xffffu, bytes, 0x00020000u}; }
DI u32 ldsaddr(const char* p) { return (u32)(unsigned long long)(LAS const char*)p; }
DI void bar() { __builtin_amdgcn_s_barrier(); }

template <int CNT, int K> DI void tail_waits() { waitv<CNT * K>(); bar(); if constexpr (K > 0) tail_waits<CNT, K - 1>(); }
template <int CNT, int NS, int NST, int SLOT, int SPLIT = NS, int OFF0 = 0, int OFF1 = 0>
struct Loader {
  u32x4 srd; u32 lb; u32 voff[CNT], sstep[CNT], ldst[CNT]; int rot;
  static DI u32 slot_off(int j) { return (u32)(j * SLOT + (j < SPLIT ? OFF0 : OFF1)); }
  DI void issue(int st, int j) {
    int sr = st + rot; if (sr >= NST) sr -= NST;
    const u32 slot = slot_off(j);
#pragma unroll
    for (int i = 0; i < CNT; ++i) dma16(srd, lb + slot + ldst[i], voff[i], sr * sstep[i]);
  }
  template <int S0, int S1> DI void prologue() {
#pragma unroll
    for (int s = S0; s < S1; ++s) issue(s, s);
  }
  DI void run() {
    static_assert(CNT * (NS - 3) <= 63, "vmcnt");
    waitv<CNT * (NS - 3)>();
    bar();
    issue(NS - 2, NS - 2);
    int j = NS - 1;
    for (int n = 0; n + NS - 1 < NST; ++n) {
      waitv<CNT * (NS - 3)>();
      bar();
      issue(n + NS - 1, j);
      j = j == NS - 1 ? 0 : j + 1;
    }
    tail_waits<CNT, NS - 3>();
  }
};

constexpr int G1_NS = 6, G1_SLOT = 22 * 1024, G1_NST = 20;
#define G2NS 12
constexpr int G2_NS = G2NS, G2_SLOT = 10 * 1024, G2_RING = 2 * G1_SLOT, G2_NST = 27;
DI constexpr int g2_off(int j) { return j < 8 ? G2_RING + j * G2_SLOT : (j - 8) * G2_SLOT; }
#define G2HW 9
constexpr int G2_HW = G2HW;
constexpr int G_AUX = G1_NS * G1_SLOT, G_LDS = G_AUX + 11 * 1024;
static_assert(G2_RING + 8 * G2_SLOT <= G_AUX && (G2_NS - 8) * G2_SLOT <= G2_RING, "phase B ring lies in the slots that phase A has left");
typedef __attribute__((address_space(1))) u32 gu32;

template <int CNTA, int CNTB>
DI void mlp_loader(char* sm, char* ws, int rt, int c, int rt3, int dg, int rotA, int rotB, int wi, int lw, int lane) {
  const u32x4 srd = mksrd(ws, WS_END);
  const u32 lb = ldsaddr(sm);
  {
#pragma unroll
    for (int i = 0; i < 3; ++i) {
      const int p = lw + 4 * i;
      if (p < 10) dma16(srd, lb + G_AUX + p * 1024, WS_HT + (80 * dg + 8 * p + (lane >> 3)) * (S * 2) + rt3 * 128 + (lane & 7) * 16, 0);
    }
    Loader<CNTA, G1_NS, G1_NST, G1_SLOT> L;
    L.srd = srd; L.lb = lb; L.rot = rotA;
    const int l15 = lane & 15, q = lane >> 4;
#pragma unroll
    for (int i = 0; i < CNTA; ++i) {
      const int p = lw + 4 * i;
      L.ldst[i] = p * 1024;
      if (p < 14) { L.voff[i] = WS_W1T + (c * 20 * 14 + p) * 1024 + lane * 16; L.sstep[i] = 14 * 1024; }
      else { const int kk = 4 * (p - 14) + q, f = (kk & 3) | (((kk >> 3) & 1) << 2);
             L.voff[i] = WS_HT + kk * (S * 2) + rt * 256 + 16 * (l15 ^ (f << 1)); L.sstep[i] = 32 * S * 2; }
    }
    L.template prologue<0, G1_NS - 2>();
    L.run();
  }
  Loader<CNTB, G2_NS, G2_NST, G2_SLOT, 8, G2_RING, -8 * G2_SLOT> L;
  L.srd = srd; L.lb = lb; L.rot = rotB;
#pragma unroll
  for (int i = 0; i < CNTB; ++i) { const int p = lw + 4 * i; L.ldst[i] = p * 1024; L.voff[i] = WS_W2T + (dg * 54 * 5 + p) * 1024 + lane * 16; L.sstep[i] = 10 * 1024;
  }
  L.template prologue<0, (G2_NS - 2 < 8 ? G2_NS - 2 : 8)>();
  bar();
  L.template prologue<8, G2_NS - 2>();
  L.run();
  __syncthreads();
  __syncthreads();
}

template <int NT, int T0, bool STAT>
DI void g1_body(char* sm, char* ws, int rt, int c, int w, int lane) {
  const int tw = w & 3, l15 = lane & 15, q = lane >> 4;
  const int qp = l15 >> 2, p4 = lane & 3, msk = 2 * qp | ((q & 1) << 3);
  int hoff[2];
#pragma unroll
  for (int i = 0; i < 2; ++i) hoff[i] = 14336 + (8 * q + qp) * 256 + 16 * ((4 * tw + 2 * i + (p4 >> 1)) ^ msk) + 8 * (p4 & 1);
  const int woff = T0 * 1024 + lane * 16;
  u32x4 W[NT];
  u32x2 h0a, h0b, h1a, h1b;
  f32x4 acc[2][NT], accS[2], accG[2];
#pragma unroll
  for (int i = 0; i < 2; ++i) {
    accS[i] = f32x4{0, 0, 0, 0}; accG[i] = f32x4{0, 0, 0, 0};
#pragma unroll
    for (int m = 0; m < NT; ++m) acc[i][m] = f32x4{0, 0, 0, 0};
  }
  const u32x4 ones = {0x3f803f80u, 0x3f803f80u, 0x3f803f80u, 0x3f803f80u};
  auto half0 = [&](const char* slot) {
    const u32x4 h0 = {h0a[0], h0a[1], h0b[0], h0b[1]};
#pragma unroll
    for (int m = 0; m < NT; ++m) {
      acc[0][m] = mfma16(W[m], h0, acc[0][m]);
      if (m == 0) h1a = trd(slot + hoff[1]);
      if (m == 1) h1b = trd(slot + hoff[1] + 1024);
      pin();
    }
    if (STAT) { accS[0] = mfma16(ones, h0, accS[0]); accG[0] = mfma16(h0, h0, accG[0]); pin(); }
  };
  auto half1 = [&](const char* nslot, bool doread) {
    const u32x4 h1 = {h1a[0], h1a[1], h1b[0], h1b[1]};
#pragma unroll
    for (int m = 0; m < NT; ++m) {
      acc[1][m] = mfma16(W[m], h1, acc[1][m]);
      if (doread) {
        W[m] = *(const u32x4*)(nslot + woff + m * 1024);
        if (m == 0) h0a = trd(nslot + hoff[0]);
        if (m == 1) h0b = trd(nslot + hoff[0] + 1024);
      }
      pin();
    }
    if (STAT) { accS[1] = mfma16(ones, h1, accS[1]); accG[1] = mfma16(h1, h1, accG[1]); pin(); }
  };
  bar();
#pragma unroll
  for (int m = 0; m < NT; ++m) {
    W[m] = *(const u32x4*)(sm + woff + m * 1024);
    if (m == 0) h0a = trd(sm + hoff[0]);
    if (m == 1) h0b = trd(sm + hoff[0] + 1024);
  }
  u32 cur = 0;
  half0(sm);
  for (int n = 0; n < G1_NST - 1; ++n) {
    bar();
    cur = cur == (G1_NS - 1) * G1_SLOT ? 0u : cur + G1_SLOT;
    half1(sm + cur, true);
    half0(sm + cur);
  }
  half1(sm, false);
  const rsrc_t rs = mkrsrc(ws, WS_END);
  const int ksb = 3 * c;
#pragma unroll
  for (int i = 0; i < 2; ++i) {
    const u32 ttile = 8 * rt + 2 * tw + i;
    u32 o[NT / 2][2];
#pragma unroll
    for (int pr = 0; pr < NT / 2; ++pr) {
      const f32x4 g = acc[i][2 * pr], u = acc[i][2 * pr + 1];
      o[pr][0] = pk2(fmaxf(g[0], 0.f) * u[0], fmaxf(g[1], 0.f) * u[1]); o[pr][1] = pk2(fmaxf(g[2], 0.f) * u[2], fmaxf(g[3], 0.f) * u[3]);
    }
    if (NT == 8) {
      st128(rs, WS_HM + ((ttile * 54 + ksb) * 64 + lane) * 16, u32x4{o[0][0], o[0][1], o[1][0], o[1][1]});
      st128(rs, WS_HM + ((ttile * 54 + ksb + 1) * 64 + lane) * 16, u32x4{o[2 % (NT / 2)][0], o[2 % (NT / 2)][1], o[3 % (NT / 2)][0], o[3 % (NT / 2)][1]});
    } else {
      st128(rs, WS_HM + ((ttile * 54 + ksb + 2) * 64 + lane) * 16, u32x4{o[0][0], o[0][1], o[1][0], o[1][1]});
      if (NT == 6) st64(rs, WS_HM + ((ttile * 54 + 48 + (c >> 1)) * 64 + lane) * 16 + 8 * (c & 1), u32x2{o[2 % (NT / 2)][0], o[2 % (NT / 2)][1]});
    }
    if (STAT) {
      if (tw == c - 12 && q == (l15 >> 2)) {
        const int r = l15 & 3;
        const float s2 = r == 0 ? accG[i][0] : r == 1 ? accG[i][1] : r == 2 ? accG[i][2] : accG[i][3];
        const float mean = accS[i][0] * (1.f / D), var = fmaxf(s2 * (1.f / D) - mean * mean, 0.f);
        const float rinv = __builtin_amdgcn_rcpf(rbf(__builtin_amdgcn_sqrtf(rbf(rbf(var) + EPS))));
        __builtin_amdgcn_raw_buffer_store_b32(__float_as_uint(rinv * rinv), rs, WS_R2 + (16 * ttile + l15) * 4, 0, AUX_WT);
      }
    }
  }
}

DI void g2_body(char* sm, char* ws, u16* out, int rt3, int dg, int rot, int w, int lane STAMP_PARAM) {
  const int tt = w & 3, kh = w >> 2, l15 = lane & 15, q = lane >> 4;
  const rsrc_t rs = mkrsrc(ws, WS_END);
  const u32 hbase = WS_HM + ((4 * rt3 + tt) * 54 + kh) * 1024 + lane * 16;
  u32x4 H[G2_HW];
  auto hload = [&](int n) { int sr = n + rot; if (sr >= G2_NST) sr -= G2_NST;
    return __builtin_amdgcn_raw_buffer_load_b128(rs, hbase, sr * 2048, AUX_WT);
  };
#pragma unroll
  for (int j = 0; j < G2_HW; ++j) H[j] = hload(j);
  const float r2 = __uint_as_float(__builtin_amdgcn_raw_buffer_load_b32(rs, WS_R2 + (64 * rt3 + 16 * tt + l15) * 4, 0, AUX_WT));
  pin();
  const char* wb = sm + kh * 5 * 1024 + lane * 16;
  u32x4 Wa[5], Wb[5];
  f32x4 acc[5];
#pragma unroll
  for (int m = 0; m < 5; ++m) { acc[m] = f32x4{0, 0, 0, 0}; Wa[m] = *(const u32x4*)(wb + g2_off(0) + m * 1024); }
  auto step = [&](u32x4 (&Wc)[5], u32x4 (&Wn)[5], int n) {
    if (n + 1 < G2_NST) bar();
#pragma unroll
    for (int m = 0; m < 5; ++m) {
      if (n + 1 < G2_NST) Wn[m] = *(const u32x4*)(wb + g2_off((n + 1) % G2_NS) + m * 1024);
      acc[m] = mfma16(Wc[m], H[n % G2_HW], acc[m]);
      pin();
    }
    if (n + G2_HW < G2_NST) { H[n % G2_HW] = hload(n + G2_HW); pin(); }
  };
#pragma unroll
  for (int n = 0; n < G2_NST; ++n) { if (n & 1) step(Wb, Wa, n); else step(Wa, Wb, n);
  }
  ST(4);
  __syncthreads();
  if (kh == 1) {
#pragma unroll
    for (int m = 0; m < 5; ++m) *(f32x4*)(sm + (tt * 5 + m) * 1024 + lane * 16) = acc[m];
  }
  __syncthreads();
  if (kh == 0) {
    const u16* res = (const u16*)(sm + G_AUX);
#pragma unroll
    for (int m = 0; m < 5; ++m) {
      const f32x4 o = acc[m] + *(const f32x4*)(sm + (tt * 5 + m) * 1024 + lane * 16);
      float r[4];
#pragma unroll
      for (int j = 0; j < 4; ++j) r[j] = __uint_as_float((u32)res[(16 * m + 4 * q + j) * 64 + 16 * tt + l15] << 16) + rbf(o[j] * r2);
      *(u32x2*)(out + (64 * rt3 + 16 * tt + l15) * D + 80 * dg + 16 * m + 4 * q) = u32x2{pk2(r[0], r[1]), pk2(r[2], r[3])};
    }
  }
}

DI void mlp_warm(char* sm, char* ws, int dg, int i, int lane) {
  const u32x4 srd = mksrd(ws, WS_END);
  const u32 lb = ldsaddr(sm) + G_AUX + 10 * 1024;
  const u32 voff = WS_W2T + (dg >> 2) * (4 * 54 * 5 * 1024) + i * 34560 + lane * 16;
  bar();
  for (int n = 0; n < 17; ++n) { bar(); dma16(srd, lb, voff, n * 2048); dma16(srd, lb, voff, n * 2048 + 1024); }
  waitv<0>();
}
constexpr int MLP_THREADS = 768;
extern "C" __global__ __launch_bounds__(MLP_THREADS) void k2_mlp(char* __restrict__ ws, u16* __restrict__ out) {
  extern __shared__ __attribute__((aligned(16))) char sm[];
  const int b = blockIdx.x, x = b & 7, i = b >> 3, cq = x >> 1;
  const int rt = 8 * (x & 1) + (i & 7), c = 4 * cq + (i >> 3);
  const int rt3 = 2 * rt + (cq >> 1), dg = 4 * (cq & 1) + (c & 3);
  const int lane = threadIdx.x & 63, w = __builtin_amdgcn_readfirstlane(threadIdx.x >> 6);
#define ROTA 0
#define ROTB 0
  const int rotA = ROTA ? (5 * (c & 3) + (5 * (i & 7)) / 2) % G1_NST : 0, rotB = ROTB ? (7 * (dg & 3) + (27 * (i & 7)) / 8) % G2_NST : 0;
  if (w == 12) { mlp_warm(sm, ws, dg, i, lane); return; }
  if (w >= 8) {
    if (w < 10) mlp_loader<6, 3>(sm, ws, rt, c, rt3, dg, rotA, rotB, i, w - 8, lane); else mlp_loader<5, 2>(sm, ws, rt, c, rt3, dg, rotA, rotB, i, w - 8, lane);
    return;
  }
  STAMP_DECL ST(0);
  if (w < 4) g1_body<8, 0, false>(sm, ws, rt, c, w, lane);
  else if (c < 12) g1_body<6, 8, false>(sm, ws, rt, c, w, lane);
  else g1_body<4, 8, true>(sm, ws, rt, c, w, lane);
  ST(1);
  asm volatile("s_waitcnt vmcnt(0)" ::: "memory");
  bar();
  ST(2);
  if (threadIdx.x == 0) {
    gu32* cnt = (gu32*)(ws + WS_CNT) + (rt3 >> 1) * 32;
    __hip_atomic_fetch_add((gu32*)(ws + WS_CNT) + rt * 32, 1u, __ATOMIC_RELAXED, __HIP_MEMORY_SCOPE_AGENT);
    for (u32 spins = 0; __hip_atomic_load(cnt, __ATOMIC_RELAXED, __HIP_MEMORY_SCOPE_AGENT) < 16u && spins < (1u << 20); ++spins) __builtin_amdgcn_s_sleep(1);
  }
  __builtin_amdgcn_fence(__ATOMIC_ACQUIRE, "wavefront");
  bar();
  ST(3);
  g2_body(sm, ws, out, rt3, dg, rotB, w, lane STAMP_PASS);
  ST(5); STAMP_OUT(2);
}

extern "C" __global__ void k9_end() {}

extern "C" void kernel_launch(void* const* d_in, const int* in_sizes, int n_in, void* d_out, int out_size, void* d_ws, size_t ws_size, hipStream_t stream) {
  const u16* q = (const u16*)d_in[0]; const u16* k = (const u16*)d_in[1]; const u16* v = (const u16*)d_in[2];
  const u16* shk = (const u16*)d_in[3]; const u16* ssmk = (const u16*)d_in[4]; const u16* ssmD = (const u16*)d_in[5];
  const u16* wg = (const u16*)d_in[7]; const u16* wu = (const u16*)d_in[8]; const u16* wd = (const u16*)d_in[9];
  char* ws = (char*)d_ws;
  (void)hipFuncSetAttribute((const void*)k2_mlp, hipFuncAttributeMaxDynamicSharedMemorySize, G_LDS);
  k0_prep<<<448, 256, 0, stream>>>(q, k, v, shk, ws);
  k1_conv<<<D + NW1 + NW2, 256, K1_LDS, stream>>>(ssmk, ssmD, wg, wu, wd, ws);
  k2_mlp<<<256, MLP_THREADS, G_LDS, stream>>>(ws, (u16*)d_out);
}
```

```cpp
#include <hip/hip_runtime.h>
#include <stdint.h>
#define NO_WARM 1

typedef unsigned short u16;
typedef unsigned int u32;
typedef u32 u32x2 __attribute__((ext_vector_type(2)));
typedef u32 u32x4 __attribute__((ext_vector_type(4)));
typedef short s16x4 __attribute__((ext_vector_type(4)));
typedef short s16x8 __attribute__((ext_vector_type(8)));
typedef float f32x2 __attribute__((ext_vector_type(2)));
typedef float f32x4 __attribute__((ext_vector_type(4)));
typedef __bf16 bf16x2 __attribute__((ext_vector_type(2)));
#define LAS __attribute__((address_space(3)))
#define DI __device__ __forceinline__

constexpr int S = 2048, D = 640, F = 1728;
constexpr float EPS = 1e-5f;

constexpr u32 WS_URT = 0;
constexpr u32 WS_XT  = WS_URT + D * S * 2;
constexpr u32 WS_HT  = WS_XT + D * S * 2;
constexpr u32 WS_ST1 = WS_HT + D * S * 2;
constexpr u32 WS_R2  = WS_ST1 + S * 4;
constexpr u32 WS_CNT = WS_R2 + S * 4;
constexpr u32 WS_W1T = WS_CNT + 16384;
constexpr u32 WS_W2T = WS_W1T + 16 * 20 * 14 * 1024;
constexpr u32 WS_HM  = WS_W2T + 8 * 54 * 5 * 1024;
constexpr u32 WS_END = WS_HM + 128 * 54 * 1024;

typedef __amdgpu_buffer_rsrc_t rsrc_t;
DI float blo(u32 u) { return __uint_as_float(u << 16); }
DI float bhi(u32 u) { return __uint_as_float(u & 0xffff0000u); }
DI u32 pk2(float a, float b) { bf16x2 r; r[0] = (__bf16)a; r[1] = (__bf16)b; return __builtin_bit_cast(u32, r); }
DI float rbf(float a) { return blo(pk2(a, a)); }
DI f32x4 mfma16(u32x4 a, u32x4 b, f32x4 c) {
  return __builtin_amdgcn_mfma_f32_16x16x32_bf16(__builtin_bit_cast(s16x8, a), __builtin_bit_cast(s16x8, b), c, 0, 0, 0);
}
DI rsrc_t mkrsrc(const void* p, u32 bytes) { return __builtin_amdgcn_make_buffer_rsrc((void*)p, 0, bytes, 0x00020000); }
constexpr int AUX_WT = 16, AUX_NT = 2;
DI u32x4 ld128nt(rsrc_t rs, u32 off) { return __builtin_amdgcn_raw_buffer_load_b128(rs, off, 0, AUX_NT); }
DI u32x4 ld128(rsrc_t rs, u32 off) { return __builtin_amdgcn_raw_buffer_load_b128(rs, off, 0, 0); }
DI u32x2 ld64(rsrc_t rs, u32 off) { return __builtin_amdgcn_raw_buffer_load_b64(rs, off, 0, 0); }
template <int AUX = AUX_WT> DI void st128(rsrc_t rs, u32 off, u32x4 v) { __builtin_amdgcn_raw_buffer_store_b128(v, rs, off, 0, AUX); }
template <int AUX = AUX_WT> DI void st64(rsrc_t rs, u32 off, u32x2 v) { __builtin_amdgcn_raw_buffer_store_b64(v, rs, off, 0, AUX); }
constexpr int P_HT = 0, P_K0 = 0, P_W = 16;
DI void pin() { __builtin_amdgcn_sched_barrier(0); }
DI u32x2 trd(const char* p) { return __builtin_bit_cast(u32x2, __builtin_amdgcn_ds_read_tr16_b64_v4i16((LAS s16x4*)p)); }
DI u32x4 tr_frag(const char* p0, const char* p1) { const u32x2 a = trd(p0), b = trd(p1); return u32x4{a[0], a[1], b[0], b[1]}; }

#define STAMP_DECL
#define STAMP_PARAM
#define STAMP_PASS
#define ST(i) do {} while (0)
#define STAMP_OUT(K) do {} while (0)

extern "C" __global__ __launch_bounds__(256) void k0_prep(const u16* __restrict__ x, const u16* __restrict__ k, const u16* __restrict__ v, const u16* __restrict__ shk, char* __restrict__ ws) {
  __shared__ __attribute__((aligned(16))) u32 sm[2 * 64 * 32];
  const int b = blockIdx.x, tid = threadIdx.x;
  STAMP_DECL ST(0);
  asm volatile("" :: "s"(x), "s"(k), "s"(v), "s"(shk), "s"(ws));
  const rsrc_t rx = mkrsrc(x, S * D * 2);
  if (__builtin_expect(b >= 320, 0)) {
  if (b == 447 && tid < 16) ((u32*)(ws + WS_CNT))[tid * 32] = 0u;
  if (b == 446) {
    unsigned long long c0, c1;
    asm volatile("s_getpc_b64 s[40:41]\n\ts_add_u32 s40, s40, k1_conv@rel32@lo+4\n\ts_addc_u32 s41, s41, k1_conv@rel32@hi+12\n\ts_mov_b64 %0, s[40:41]\n\t"
                 "s_getpc_b64 s[40:41]\n\ts_add_u32 s40, s40, k9_end@rel32@lo+4\n\ts_addc_u32 s41, s41, k9_end@rel32@hi+12\n\ts_mov_b64 %1, s[40:41]"
                 : "=s"(c0), "=s"(c1) :: "s40", "s41", "scc");
    const rsrc_t rc = mkrsrc((const void*)c0, (u32)(c1 - c0));
    const u32 p0 = __builtin_amdgcn_raw_buffer_load_b32(rc, tid * 128, 0, 0), p1 = __builtin_amdgcn_raw_buffer_load_b32(rc, (tid + 256) * 128, 0, 0);
    unsigned long long kd;
    asm volatile("s_getpc_b64 s[40:41]\n\ts_add_u32 s40, s40, k0_prep.kd@rel32@lo+4\n\ts_addc_u32 s41, s41, k0_prep.kd@rel32@hi+12\n\ts_mov_b64 %0, s[40:41]" : "=s"(kd) :: "s40", "s41", "scc");
    const u32 p2 = __builtin_amdgcn_raw_buffer_load_b32(mkrsrc((const void*)(kd & ~127ull), 512), (tid & 3) * 128, 0, 0);
    const unsigned long long ka = (unsigned long long)__builtin_amdgcn_kernarg_segment_ptr() & ~4095ull;
    const u32 p3 = __builtin_amdgcn_raw_buffer_load_b32(mkrsrc((const void*)ka, 4096), (tid & 31) * 128, 0, 0);
    asm volatile("" :: "v"(p0), "v"(p1), "v"(p2), "v"(p3));
  }
    const int sb = b - 320, row = 64 * ((sb & 7) + 8 * (sb >> 5)) + 16 * ((sb >> 3) & 3) + (tid >> 4), s = tid & 15;
    u32x4 r[5];
#pragma unroll
    for (int i = 0; i < 5; ++i) r[i] = ld128nt(rx, (row * D + 8 * (s + 16 * i)) * 2);
    pin();
    float sum = 0.f;
#pragma unroll
    for (int i = 0; i < 5; ++i)
#pragma unroll
      for (int j = 0; j < 4; ++j) sum += blo(r[i][j]) + bhi(r[i][j]);
#pragma unroll
    for (int o = 1; o < 16; o <<= 1) sum += __shfl_xor(sum, o);
    const float mean = sum * (1.f / D);
    float q = 0.f;
#pragma unroll
    for (int i = 0; i < 5; ++i)
#pragma unroll
      for (int j = 0; j < 4; ++j) { const float a = blo(r[i][j]) - mean, c2 = bhi(r[i][j]) - mean; q += a * a + c2 * c2; }
#pragma unroll
    for (int o = 1; o < 16; o <<= 1) q += __shfl_xor(q, o);
    const float sd = __builtin_amdgcn_sqrtf(rbf(rbf(q * (1.f / D)) + EPS));
    if (s == 0) ((u32*)(ws + WS_ST1))[row] = pk2(sd, mean);
      ST(2); STAMP_OUT(0);
    return;
  }
  {
    const rsrc_t rk = mkrsrc(k, S * D * 2), rv = mkrsrc(v, S * D * 2), rw = mkrsrc(shk, D * 8);
    const int tb = (b & 7) + 8 * ((b >> 3) / 10), cb = (b >> 3) % 10, c = tid & 7, rg = tid >> 3;
    const int t = 64 * tb + 2 * rg, ch0 = 64 * cb + 8 * c;
    const u32 o = (u32)(t * D + ch0) * 2u;
    u32x4 kr[5], vr[2], xr[2], w[4];
#pragma unroll
    for (int i = 0; i < 5; ++i) kr[i] = ld128nt(rk, o + (u32)((i - 3) * D * 2));
#pragma unroll
    for (int i = 0; i < 2; ++i) { vr[i] = ld128nt(rv, o + i * D * 2); xr[i] = ld128nt(rx, o + i * D * 2); }
#pragma unroll
    for (int i = 0; i < 4; ++i) w[i] = ld128(rw, ch0 * 8 + 16 * i);
    pin();
    u32* sU = sm; u32* sX = sm + 64 * 32;
    const int colU = 31 - rg, colX = rg;
    const int pU = 4 * ((colU >> 2) ^ c) + (colU & 3), pX = 4 * ((colX >> 2) ^ c) + (colX & 3);
#pragma unroll
    for (int j = 0; j < 8; ++j) {
      float kk[5];
#pragma unroll
      for (int i = 0; i < 5; ++i) kk[i] = (j & 1) ? bhi(kr[i][j >> 1]) : blo(kr[i][j >> 1]);
      const u32 wa = w[j >> 1][(j & 1) * 2], wb = w[j >> 1][(j & 1) * 2 + 1];
      const float w0 = blo(wa), w1 = bhi(wa), w2 = blo(wb), w3 = bhi(wb);
      const float s0 = kk[0] * w3 + kk[1] * w2 + kk[2] * w1 + kk[3] * w0;
      const float s1 = kk[1] * w3 + kk[2] * w2 + kk[3] * w1 + kk[4] * w0;
      const u32 sp = pk2(s0, s1);
      const float v0 = (j & 1) ? bhi(vr[0][j >> 1]) : blo(vr[0][j >> 1]);
      const float v1 = (j & 1) ? bhi(vr[1][j >> 1]) : blo(vr[1][j >> 1]);
      sU[(8 * c + j) * 32 + pU] = pk2(bhi(sp) * v1, blo(sp) * v0);
      sX[(8 * c + j) * 32 + pX] = (j & 1) ? __builtin_amdgcn_perm(xr[1][j >> 1], xr[0][j >> 1], 0x07060302u) : __builtin_amdgcn_perm(xr[1][j >> 1], xr[0][j >> 1], 0x05040100u);
    }
    ST(1);
    __syncthreads();
#pragma unroll
    for (int i = 0; i < 2; ++i) {
      const int piece = tid + 256 * i, ch = piece >> 3, g = piece & 7, p = ch * 32 + 4 * (g ^ (ch >> 3));
      st128<P_K0>(mkrsrc(ws, WS_END), WS_URT + ((64 * cb + ch) * S + (1984 - 64 * tb) + 8 * g) * 2, *(const u32x4*)(sU + p));
      st128<P_K0>(mkrsrc(ws, WS_END), WS_XT + ((64 * cb + ch) * S + 64 * tb + 8 * g) * 2, *(const u32x4*)(sX + p));
    }
  }
  ST(2); STAMP_OUT(0);
}

constexpr int NW1 = 112;
constexpr int NTASK2 = 540, TPW = 2, NW2 = (NTASK2 + 4 * TPW - 1) / (4 * TPW);
constexpr int KC_STRIDE = 4160;
constexpr int K1_U = 32 * 1024, K1_LDS = K1_U + 2304 * 2;

DI void w1_strip(int r, const u16* wg, const u16* wu, char* ws, char* sm) {
  const int p = 8 * (r >> 4) + (r & 7), s = 2 * p + ((r >> 3) & 1);
  if (s >= 108) return;
  const int tid = threadIdx.x, lane = tid & 63, w = __builtin_amdgcn_readfirstlane(tid >> 6);
  const int m = s / 54, col0 = 32 * (s % 54), c4 = tid & 3, kg = tid >> 2;
  const rsrc_t rw = mkrsrc(m ? wu : wg, D * F * 2), rs = mkrsrc(ws, WS_END);
  u32x4 rr[10];
#pragma unroll
  for (int i = 0; i < 10; ++i) rr[i] = ld128nt(rw, ((kg + 64 * i) * F + col0 + 8 * c4) * 2);
  pin();
  const int g = lane >> 4, qp = (lane & 15) >> 2, p4 = lane & 3, c2 = w & 1;
  const u32x4 ones = {0x3f803f80u, 0x3f803f80u, 0x3f803f80u, 0x3f803f80u};
  u32x4 fr[10];
  f32x4 cs = {0, 0, 0, 0};
#pragma unroll
  for (int hf = 0; hf < 2; ++hf) {
    if (hf) __syncthreads();
#pragma unroll
    for (int i = 0; i < 5; ++i) *(u32x4*)(sm + (kg + 64 * i) * 80 + 16 * c4) = rr[5 * hf + i];
    __syncthreads();
#pragma unroll
    for (int i = 0; i < 5; ++i) {
      const char* im = sm + (32 * ((w >> 1) + 2 * i) + 8 * g + qp) * 80 + 32 * c2 + 8 * p4;
      fr[5 * hf + i] = tr_frag(im, im + 4 * 80);
      cs = mfma16(ones, fr[5 * hf + i], cs);
    }
  }
  float* red = (float*)(sm + 320 * 80);
  if (lane < 16) red[w * 16 + lane] = cs[0];
  __syncthreads();
  const float mu = (red[w * 16 + (lane & 15)] + red[(w ^ 2) * 16 + (lane & 15)]) * (1.f / D);
  const int ft = (col0 >> 4) + c2;
  const int c = ft < 84 ? ft / 7 : 12 + (ft - 84) / 6, pair = ft < 84 ? ft % 7 : (ft - 84) % 6;
#pragma unroll
  for (int i = 0; i < 10; ++i) {
    const int ks = 10 * (i / 5) + (w >> 1) + 2 * (i % 5);
    const u32x4 a = fr[i];
    st128<P_W>(rs, WS_W1T + ((c * 20 + ks) * 14 + 2 * pair + m) * 1024 + lane * 16,
          u32x4{pk2(blo(a[0]) - mu, bhi(a[0]) - mu), pk2(blo(a[1]) - mu, bhi(a[1]) - mu), pk2(blo(a[2]) - mu, bhi(a[2]) - mu), pk2(blo(a[3]) - mu, bhi(a[3]) - mu)});
  }
}
DI void w2_tasks(int pb, const u16* wd, char* ws, char* sm) {
  const int tid = threadIdx.x, lane = tid & 63, w = __builtin_amdgcn_readfirstlane(tid >> 6);
  const rsrc_t rw = mkrsrc(wd, D * F * 2), rs = mkrsrc(ws, WS_END);
  char* im = sm + w * (32 * 144);
  u32x4 ld[TPW][4];
  int task[TPW];
#pragma unroll
  for (int u = 0; u < TPW; ++u) {
    int t = (pb * 4 + w) * TPW + u; if (t >= NTASK2) t = NTASK2 - 1;
    task[u] = t;
    const int ks = t / 10, dr = t % 10;
    int lo, hi;
    if (ks < 36) { lo = 7 * (ks / 3) + 2 * (ks % 3); hi = lo + 1; } else if (ks < 48) { lo = 84 + 2 * (ks - 36); hi = lo + 1; } else { lo = 14 * (ks - 48) + 6; hi = lo + 7; }
#pragma unroll
    for (int i = 0; i < 4; ++i) { const int r = (lane >> 3) + 8 * i; ld[u][i] = ld128nt(rw, ((16 * (r < 16 ? lo : hi) + (r & 15)) * D + 64 * dr + 8 * (lane & 7)) * 2); }
  }
  pin();
  const int g = lane >> 4, qp = (lane & 15) >> 2, p4 = lane & 3;
#pragma unroll
  for (int u = 0; u < TPW; ++u) {
#pragma unroll
    for (int i = 0; i < 4; ++i) *(u32x4*)(im + ((lane >> 3) + 8 * i) * 144 + 16 * (lane & 7)) = ld[u][i];
    const int ks = task[u] / 10;
#pragma unroll
    for (int c4 = 0; c4 < 4; ++c4) {
      const int dt = 4 * (task[u] % 10) + c4;
      const u32x4 fr = tr_frag(im + (4 * g + qp) * 144 + 32 * c4 + 8 * p4, im + (16 + 4 * g + qp) * 144 + 32 * c4 + 8 * p4);
      st128<P_W>(rs, WS_W2T + (((dt / 5) * 54 + ks) * 5 + dt % 5) * 1024 + lane * 16, fr);
    }
  }
}

extern "C" __global__ __launch_bounds__(256, 4) void k1_conv(const u16* __restrict__ ssmk, const u16* __restrict__ ssmD, const u16* __restrict__ wg, const u16* __restrict__ wu, const u16* __restrict__ wd, char* __restrict__ ws) {
  extern __shared__ __attribute__((aligned(16))) char sm[];
  const int b = blockIdx.x, tid = threadIdx.x;
  STAMP_DECL ST(0);
  asm volatile("" :: "s"(ssmk), "s"(ssmD), "s"(ws));
  if (__builtin_expect(b >= D, 0)) {
    if (b >= D + NW1) w2_tasks(b - D - NW1, wd, ws, sm); else w1_strip(b - D, wg, wu, ws, sm);
    ST(5); STAMP_OUT(1); return;
  }
  const int d = b, lane = tid & 63, w = __builtin_amdgcn_readfirstlane(tid >> 6);
  const int l15 = lane & 15, q = lane >> 4;
  const rsrc_t rk = mkrsrc(ssmk + d * S, S * 2), rs = mkrsrc(ws, WS_END);
  const u32x4 c0 = ld128nt(rk, 16 * tid), c1 = ld128nt(rk, 16 * tid + 16);
  const u32x4 uu = ld128(rs, WS_URT + d * (S * 2) + 16 * tid);
  u32x2 xv[2]; u32x4 st[2];
#pragma unroll
  for (int i = 0; i < 2; ++i) {
    const int t = 256 * (2 * w + i) + 16 * l15 + 4 * q;
    xv[i] = ld64(rs, WS_XT + (d * S + t) * 2);
    st[i] = ld128(rs, WS_ST1 + t * 4);
  }
  const float Dd = blo(ssmD[d]);
  pin();
  ST(1);
  auto build = [&](int m, const u32x4& a, const u32x4& bb) {
    const u32 ww[8] = {a[0], a[1], a[2], a[3], bb[0], bb[1], bb[2], bb[3]};
    u32x4 o0, o1, o2, o3;
#pragma unroll
    for (int i = 0; i < 4; ++i) {
      o0[i] = __builtin_amdgcn_alignbyte(ww[i + 1], ww[i], 2); o1[i] = ww[i + 1];
      o2[i] = __builtin_amdgcn_alignbyte(ww[i + 2], ww[i + 1], 2); o3[i] = ww[i + 2];
    }
    *(u32x4*)(sm + 0 * KC_STRIDE + 16 * m) = o0; *(u32x4*)(sm + 1 * KC_STRIDE + 16 * m) = o1;
    *(u32x4*)(sm + 2 * KC_STRIDE + 16 * m) = o2; *(u32x4*)(sm + 3 * KC_STRIDE + 16 * m) = o3;
  };
  const u32x4 zero4 = {0, 0, 0, 0};
  build(tid + 2, c0, c1);
  if (tid < 2) build(1 - tid, zero4, tid == 0 ? c0 : zero4);
  *(u32x4*)(sm + K1_U + 16 * tid) = uu;
  if (tid < 32) *(u32x4*)(sm + K1_U + 4096 + 16 * tid) = zero4;
  __syncthreads();
  ST(2);
  f32x4 acc[8];
#pragma unroll
  for (int j = 0; j < 8; ++j) acc[j] = f32x4{0, 0, 0, 0};
#pragma unroll
  for (int rr = 0; rr < 2; ++rr) {
    const int rho = 2 * w + rr;
    const char* pf = sm + (l15 & 3) * KC_STRIDE + 2 * (l15 & 12) + 16 * q + 64 * rho;
    const char* pg = sm + K1_U + 480 - 32 * l15 + 64 * rho + 16 * q;
    u32x4 Ff[8], Gf[8];
#pragma unroll
    for (int i = 0; i < 8; ++i) {
      Gf[i] = *(const u32x4*)(pg + 512 * (7 - i));
      const u32x2 a = *(const volatile LAS u32x2*)(pf + 512 * i), bb = *(const volatile LAS u32x2*)(pf + 512 * i + 8);
      Ff[i] = u32x4{a[0], a[1], bb[0], bb[1]};
    }
#pragma unroll
    for (int i = 0; i < 8; ++i)
#pragma unroll
      for (int e = 0; e + i < 8; ++e) acc[i + e] = mfma16(Ff[i], Gf[e], acc[i + e]);
  }
  ST(3);
  __syncthreads();
  f32x4 own[2];
  {
    char* p = sm + w * 1024 + lane * 16;
#define K1_PUT(W) do { _Pragma("unroll") for (int j = 0; j < 8; ++j) { if ((j >> 1) != (W)) *(f32x4*)(p + j * 4096) = acc[j]; } own[0] = acc[2 * (W)]; own[1] = acc[2 * (W) + 1]; } while (0)
    if (w == 0) K1_PUT(0); else if (w == 1) K1_PUT(1); else if (w == 2) K1_PUT(2); else K1_PUT(3);
  }
  __syncthreads();
  ST(4);
#pragma unroll
  for (int i = 0; i < 2; ++i) {
    const int j = 2 * w + i;
    f32x4 y = own[i];
#pragma unroll
    for (int s = 1; s < 4; ++s) y += *(const f32x4*)(sm + (j * 4 + ((w + s) & 3)) * 1024 + lane * 16);
    const int t = 256 * j + 16 * l15 + 4 * q;
    const u32x2 ur = *(const u32x2*)(sm + K1_U + 2 * (2044 - t));
    const float uv[4] = {bhi(ur[1]), blo(ur[1]), bhi(ur[0]), blo(ur[0])};
    const float xx[4] = {blo(xv[i][0]), bhi(xv[i][0]), blo(xv[i][1]), bhi(xv[i][1])};
    float h[4];
#pragma unroll
    for (int r = 0; r < 4; ++r) {
      const float ln = (xx[r] - bhi(st[i][r])) * __builtin_amdgcn_rcpf(blo(st[i][r]));
      h[r] = fmaf(ln, fmaf(uv[r], Dd, y[r]), xx[r]);
    }
    st64<P_HT>(rs, WS_HT + (d * S + t) * 2, u32x2{pk2(h[0], h[1]), pk2(h[2], h[3])});
  }
  ST(5); STAMP_OUT(1);
}

template <int N> DI void waitv() { asm volatile("s_waitcnt vmcnt(%0)" ::"n"(N) : "memory"); }
DI void dma16(u32x4 srd, u32 lds, u32 voff, u32 soff) {
  asm volatile("s_mov_b32 m0, %0\n\ts_nop 0\n\tbuffer_load_dwordx4 %1, %2, %3 offen lds" ::"s"(lds), "v"(voff), "s"(srd), "s"(soff) : "memory");
}
DI u32x4 mksrd(const void* p, u32 bytes) { const unsigned long long a = (unsigned long long)p; return u32x4{(u32)a, (u32)(a >> 32) & 0xffffu, bytes, 0x00020000u}; }
DI u32 ldsaddr(const char* p) { return (u32)(unsigned long long)(LAS const char*)p; }
DI void bar() { __builtin_amdgcn_s_barrier(); }

template <int CNT, int K> DI void tail_waits() { waitv<CNT * K>(); bar(); if constexpr (K > 0) tail_waits<CNT, K - 1>(); }
template <int CNT, int NS, int NST, int SLOT, int SPLIT = NS, int OFF0 = 0, int OFF1 = 0>
struct Loader {
  u32x4 srd; u32 lb; u32 voff[CNT], sstep[CNT], ldst[CNT]; int rot;
  static DI u32 slot_off(int j) { return (u32)(j * SLOT + (j < SPLIT ? OFF0 : OFF1)); }
  DI void issue(int st, int j) {
    int sr = st + rot; if (sr >= NST) sr -= NST;
    const u32 slot = slot_off(j);
#pragma unroll
    for (int i = 0; i < CNT; ++i) dma16(srd, lb + slot + ldst[i], voff[i], sr * sstep[i]);
  }
  template <int S0, int S1> DI void prologue() {
#pragma unroll
    for (int s = S0; s < S1; ++s) issue(s, s);
  }
  DI void run() {
    static_assert(CNT * (NS - 3) <= 63, "vmcnt");
    waitv<CNT * (NS - 3)>();
    bar();
    issue(NS - 2, NS - 2);
    int j = NS - 1;
    for (int n = 0; n + NS - 1 < NST; ++n) {
      waitv<CNT * (NS - 3)>();
      bar();
      issue(n + NS - 1, j);
      j = j == NS - 1 ? 0 : j + 1;
    }
    tail_waits<CNT, NS - 3>();
  }
};

constexpr int G1_NS = 6, G1_SLOT = 22 * 1024, G1_NST = 20;
#define G2NS 12
constexpr int G2_NS = G2NS, G2_SLOT = 10 * 1024, G2_RING = 2 * G1_SLOT, G2_NST = 27;
DI constexpr int g2_off(int j) { return j < 8 ? G2_RING + j * G2_SLOT : (j - 8) * G2_SLOT; }
#define G2HW 9
constexpr int G2_HW = G2HW;
constexpr int G_AUX = G1_NS * G1_SLOT, G_LDS = G_AUX + 11 * 1024;
static_assert(G2_RING + 8 * G2_SLOT <= G_AUX && (G2_NS - 8) * G2_SLOT <= G2_RING, "phase B ring lies in the slots that phase A has left");
typedef __attribute__((address_space(1))) u32 gu32;

template <int CNTA, int CNTB>
DI void mlp_loader(char* sm, char* ws, int rt, int c, int rt3, int dg, int rotA, int rotB, int wi, int lw, int lane) {
  const u32x4 srd = mksrd(ws, WS_END);
  const u32 lb = ldsaddr(sm);
  {
    Loader<CNTA, G1_NS, G1_NST, G1_SLOT> L;
    L.srd = srd; L.lb = lb; L.rot = rotA;
    const int l15 = lane & 15, q = lane >> 4;
#pragma unroll
    for (int i = 0; i < CNTA; ++i) {
      const int p = lw + 4 * i;
      L.ldst[i] = p * 1024;
      if (p < 14) { L.voff[i] = WS_W1T + (c * 20 * 14 + p) * 1024 + lane * 16; L.sstep[i] = 14 * 1024; }
      else { const int kk = 4 * (p - 14) + q, f = (kk & 3) | (((kk >> 3) & 1) << 2);
             L.voff[i] = WS_HT + kk * (S * 2) + rt * 256 + 16 * (l15 ^ (f << 1)); L.sstep[i] = 32 * S * 2; }
    }
    L.template prologue<0, G1_NS - 2>();
    L.run();
  }
#pragma unroll
  for (int i = 0; i < 3; ++i) {
    const int p = lw + 4 * i;
    if (p < 10) dma16(srd, lb + G_AUX + p * 1024, WS_HT + (80 * dg + 8 * p + (lane >> 3)) * (S * 2) + rt3 * 128 + (lane & 7) * 16, 0);
  }
  Loader<CNTB, G2_NS, G2_NST, G2_SLOT, 8, G2_RING, -8 * G2_SLOT> L;
  L.srd = srd; L.lb = lb; L.rot = rotB;
#pragma unroll
  for (int i = 0; i < CNTB; ++i) { const int p = lw + 4 * i; L.ldst[i] = p * 1024; L.voff[i] = WS_W2T + (dg * 54 * 5 + p) * 1024 + lane * 16; L.sstep[i] = 10 * 1024;
  }
  L.template prologue<0, (G2_NS - 2 < 8 ? G2_NS - 2 : 8)>();
  bar();
  L.template prologue<8, G2_NS - 2>();
  L.run();
  __syncthreads();
  __syncthreads();
}

template <int NT, int T0, bool STAT>
DI void g1_body(char* sm, char* ws, int rt, int c, int w, int lane) {
  const int tw = w & 3, l15 = lane & 15, q = lane >> 4;
  const int qp = l15 >> 2, p4 = lane & 3, msk = 2 * qp | ((q & 1) << 3);
  int hoff[2];
#pragma unroll
  for (int i = 0; i < 2; ++i) hoff[i] = 14336 + (8 * q + qp) * 256 + 16 * ((4 * tw + 2 * i + (p4 >> 1)) ^ msk) + 8 * (p4 & 1);
  const int woff = T0 * 1024 + lane * 16;
  u32x4 W[NT];
  u32x2 h0a, h0b, h1a, h1b;
  f32x4 acc[2][NT], accS[2], accG[2];
#pragma unroll
  for (int i = 0; i < 2; ++i) {
    accS[i] = f32x4{0, 0, 0, 0}; accG[i] = f32x4{0, 0, 0, 0};
#pragma unroll
    for (int m = 0; m < NT; ++m) acc[i][m] = f32x4{0, 0, 0, 0};
  }
  const u32x4 ones = {0x3f803f80u, 0x3f803f80u, 0x3f803f80u, 0x3f803f80u};
  auto half0 = [&](const char* slot) {
    const u32x4 h0 = {h0a[0], h0a[1], h0b[0], h0b[1]};
#pragma unroll
    for (int m = 0; m < NT; ++m) {
      acc[0][m] = mfma16(W[m], h0, acc[0][m]);
      if (m == 0) h1a = trd(slot + hoff[1]);
      if (m == 1) h1b = trd(slot + hoff[1] + 1024);
      pin();
    }
    if (STAT) { accS[0] = mfma16(ones, h0, accS[0]); accG[0] = mfma16(h0, h0, accG[0]); pin(); }
  };
  auto half1 = [&](const char* nslot, bool doread) {
    const u32x4 h1 = {h1a[0], h1a[1], h1b[0], h1b[1]};
#pragma unroll
    for (int m = 0; m < NT; ++m) {
      acc[1][m] = mfma16(W[m], h1, acc[1][m]);
      if (doread) {
        W[m] = *(const u32x4*)(nslot + woff + m * 1024);
        if (m == 0) h0a = trd(nslot + hoff[0]);
        if (m == 1) h0b = trd(nslot + hoff[0] + 1024);
      }
      pin();
    }
    if (STAT) { accS[1] = mfma16(ones, h1, accS[1]); accG[1] = mfma16(h1, h1, accG[1]); pin(); }
  };
  bar();
#pragma unroll
  for (int m = 0; m < NT; ++m) {
    W[m] = *(const u32x4*)(sm + woff + m * 1024);
    if (m == 0) h0a = trd(sm + hoff[0]);
    if (m == 1) h0b = trd(sm + hoff[0] + 1024);
  }
  u32 cur = 0;
  half0(sm);
  for (int n = 0; n < G1_NST - 1; ++n) {
    bar();
    cur = cur == (G1_NS - 1) * G1_SLOT ? 0u : cur + G1_SLOT;
    half1(sm + cur, true);
    half0(sm + cur);
  }
  half1(sm, false);
  const rsrc_t rs = mkrsrc(ws, WS_END);
  const int ksb = 3 * c;
#pragma unroll
  for (int i = 0; i < 2; ++i) {
    const u32 ttile = 8 * rt + 2 * tw + i;
    u32 o[NT / 2][2];
#pragma unroll
    for (int pr = 0; pr < NT / 2; ++pr) {
      const f32x4 g = acc[i][2 * pr], u = acc[i][2 * pr + 1];
      o[pr][0] = pk2(fmaxf(g[0], 0.f) * u[0], fmaxf(g[1], 0.f) * u[1]); o[pr][1] = pk2(fmaxf(g[2], 0.f) * u[2], fmaxf(g[3], 0.f) * u[3]);
    }
    if (NT == 8) {
      st128(rs, WS_HM + ((ttile * 54 + ksb) * 64 + lane) * 16, u32x4{o[0][0], o[0][1], o[1][0], o[1][1]});
      st128(rs, WS_HM + ((ttile * 54 + ksb + 1) * 64 + lane) * 16, u32x4{o[2 % (NT / 2)][0], o[2 % (NT / 2)][1], o[3 % (NT / 2)][0], o[3 % (NT / 2)][1]});
    } else {
      st128(rs, WS_HM + ((ttile * 54 + ksb + 2) * 64 + lane) * 16, u32x4{o[0][0], o[0][1], o[1][0], o[1][1]});
      if (NT == 6) st64(rs, WS_HM + ((ttile * 54 + 48 + (c >> 1)) * 64 + lane) * 16 + 8 * (c & 1), u32x2{o[2 % (NT / 2)][0], o[2 % (NT / 2)][1]});
    }
    if (STAT) {
      if (tw == c - 12 && q == (l15 >> 2)) {
        const int r = l15 & 3;
        const float s2 = r == 0 ? accG[i][0] : r == 1 ? accG[i][1] : r == 2 ? accG[i][2] : accG[i][3];
        const float mean = accS[i][0] * (1.f / D), var = fmaxf(s2 * (1.f / D) - mean * mean, 0.f);
        const float rinv = __builtin_amdgcn_rcpf(rbf(__builtin_amdgcn_sqrtf(rbf(rbf(var) + EPS))));
        __builtin_amdgcn_raw_buffer_store_b32(__float_as_uint(rinv * rinv), rs, WS_R2 + (16 * ttile + l15) * 4, 0, AUX_WT);
      }
    }
  }
}

DI void g2_body(char* sm, char* ws, u16* out, int rt3, int dg, int rot, int w, int lane STAMP_PARAM) {
  const int tt = w & 3, kh = w >> 2, l15 = lane & 15, q = lane >> 4;
  const rsrc_t rs = mkrsrc(ws, WS_END);
  const u32 hbase = WS_HM + ((4 * rt3 + tt) * 54 + kh) * 1024 + lane * 16;
  u32x4 H[G2_HW];
  auto hload = [&](int n) { int sr = n + rot; if (sr >= G2_NST) sr -= G2_NST;
    return __builtin_amdgcn_raw_buffer_load_b128(rs, hbase, sr * 2048, AUX_WT);
  };
#pragma unroll
  for (int j = 0; j < G2_HW; ++j) H[j] = hload(j);
  const float r2 = __uint_as_float(__builtin_amdgcn_raw_buffer_load_b32(rs, WS_R2 + (64 * rt3 + 16 * tt + l15) * 4, 0, AUX_WT));
  pin();
  const char* wb = sm + kh * 5 * 1024 + lane * 16;
  u32x4 Wa[5], Wb[5];
  f32x4 acc[5];
#pragma unroll
  for (int m = 0; m < 5; ++m) { acc[m] = f32x4{0, 0, 0, 0}; Wa[m] = *(const u32x4*)(wb + g2_off(0) + m * 1024); }
  auto step = [&](u32x4 (&Wc)[5], u32x4 (&Wn)[5], int n) {
    if (n + 1 < G2_NST) bar();
#pragma unroll
    for (int m = 0; m < 5; ++m) {
      if (n + 1 < G2_NST) Wn[m] = *(const u32x4*)(wb + g2_off((n + 1) % G2_NS) + m * 1024);
      acc[m] = mfma16(Wc[m], H[n % G2_HW], acc[m]);
      pin();
    }
    if (n + G2_HW < G2_NST) { H[n % G2_HW] = hload(n + G2_HW); pin(); }
  };
#pragma unroll
  for (int n = 0; n < G2_NST; ++n) { if (n & 1) step(Wb, Wa, n); else step(Wa, Wb, n);
  }
  ST(4);
  __syncthreads();
  if (kh == 1) {
#pragma unroll
    for (int m = 0; m < 5; ++m) *(f32x4*)(sm + (tt * 5 + m) * 1024 + lane * 16) = acc[m];
  }
  __syncthreads();
  if (kh == 0) {
    const u16* res = (const u16*)(sm + G_AUX);
#pragma unroll
    for (int m = 0; m < 5; ++m) {
      const f32x4 o = acc[m] + *(const f32x4*)(sm + (tt * 5 + m) * 1024 + lane * 16);
      float r[4];
#pragma unroll
      for (int j = 0; j < 4; ++j) r[j] = __uint_as_float((u32)res[(16 * m + 4 * q + j) * 64 + 16 * tt + l15] << 16) + rbf(o[j] * r2);
      *(u32x2*)(out + (64 * rt3 + 16 * tt + l15) * D + 80 * dg + 16 * m + 4 * q) = u32x2{pk2(r[0], r[1]), pk2(r[2], r[3])};
    }
  }
}

DI void mlp_warm(char* sm, char* ws, int dg, int i, int lane) {
  const u32x4 srd = mksrd(ws, WS_END);
  const u32 lb = ldsaddr(sm) + G_AUX + 10 * 1024;
  const u32 voff = WS_W2T + (dg >> 2) * (4 * 54 * 5 * 1024) + i * 34560 + lane * 16;
  bar();
  for (int n = 0; n < 17; ++n) { bar(); dma16(srd, lb, voff, n * 2048); dma16(srd, lb, voff, n * 2048 + 1024); }
  waitv<0>();
}
constexpr int MLP_THREADS = 768;
extern "C" __global__ __launch_bounds__(MLP_THREADS) void k2_mlp(char* __restrict__ ws, u16* __restrict__ out) {
  extern __shared__ __attribute__((aligned(16))) char sm[];
  const int b = blockIdx.x, x = b & 7, i = b >> 3, cq = x >> 1;
  const int rt = 8 * (x & 1) + (i & 7), c = 4 * cq + (i >> 3);
  const int rt3 = 2 * rt + (cq >> 1), dg = 4 * (cq & 1) + (c & 3);
  const int lane = threadIdx.x & 63, w = __builtin_amdgcn_readfirstlane(threadIdx.x >> 6);
#define ROTA 0
#define ROTB 0
  const int rotA = ROTA ? (5 * (c & 3) + (5 * (i & 7)) / 2) % G1_NST : 0, rotB = ROTB ? (7 * (dg & 3) + (27 * (i & 7)) / 8) % G2_NST : 0;
  if (__builtin_expect(w >= 8, 1)) {
    if (w == 12) { mlp_warm(sm, ws, dg, i, lane); return; }
    if (w < 10) mlp_loader<6, 3>(sm, ws, rt, c, rt3, dg, rotA, rotB, i, w - 8, lane); else mlp_loader<5, 2>(sm, ws, rt, c, rt3, dg, rotA, rotB, i, w - 8, lane);
    return;
  }
  STAMP_DECL ST(0);
  if (w < 4) g1_body<8, 0, false>(sm, ws, rt, c, w, lane);
  else if (c < 12) g1_body<6, 8, false>(sm, ws, rt, c, w, lane);
  else g1_body<4, 8, true>(sm, ws, rt, c, w, lane);
  ST(1);
  asm volatile("s_waitcnt vmcnt(0)" ::: "memory");
  bar();
  ST(2);
  if (threadIdx.x == 0) {
    gu32* cnt = (gu32*)(ws + WS_CNT) + (rt3 >> 1) * 32;
    __hip_atomic_fetch_add((gu32*)(ws + WS_CNT) + rt * 32, 1u, __ATOMIC_RELAXED, __HIP_MEMORY_SCOPE_AGENT);
    for (u32 spins = 0; __hip_atomic_load(cnt, __ATOMIC_RELAXED, __HIP_MEMORY_SCOPE_AGENT) < 16u && spins < (1u << 20); ++spins) __builtin_amdgcn_s_sleep(1);
  }
  __builtin_amdgcn_fence(__ATOMIC_ACQUIRE, "wavefront");
  bar();
  ST(3);
  g2_body(sm, ws, out, rt3, dg, rotB, w, lane STAMP_PASS);
  ST(5); STAMP_OUT(2);
}

extern "C" __global__ void k9_end() {}

extern "C" void kernel_launch(void* const* d_in, const int* in_sizes, int n_in, void* d_out, int out_size, void* d_ws, size_t ws_size, hipStream_t stream) {
  const u16* q = (const u16*)d_in[0]; const u16* k = (const u16*)d_in[1]; const u16* v = (const u16*)d_in[2];
  const u16* shk = (const u16*)d_in[3]; const u16* ssmk = (const u16*)d_in[4]; const u16* ssmD = (const u16*)d_in[5];
  const u16* wg = (const u16*)d_in[7]; const u16* wu = (const u16*)d_in[8]; const u16* wd = (const u16*)d_in[9];
  char* ws = (char*)d_ws;
  (void)hipFuncSetAttribute((const void*)k2_mlp, hipFuncAttributeMaxDynamicSharedMemorySize, G_LDS);
  k0_prep<<<448, 256, 0, stream>>>(q, k, v, shk, ws);
  k1_conv<<<D + NW1 + NW2, 256, K1_LDS, stream>>>(ssmk, ssmD, wg, wu, wd, ws);
  k2_mlp<<<256, MLP_THREADS, G_LDS, stream>>>(ws, (u16*)d_out);
}
```

```cpp
#include <hip/hip_runtime.h>
#include <stdint.h>
#define NO_WARM 1

typedef unsigned short u16;
typedef unsigned int u32;
typedef u32 u32x2 __attribute__((ext_vector_type(2)));
typedef u32 u32x4 __attribute__((ext_vector_type(4)));
typedef short s16x4 __attribute__((ext_vector_type(4)));
typedef short s16x8 __attribute__((ext_vector_type(8)));
typedef float f32x2 __attribute__((ext_vector_type(2)));
typedef float f32x4 __attribute__((ext_vector_type(4)));
typedef __bf16 bf16x2 __attribute__((ext_vector_type(2)));
#define LAS __attribute__((address_space(3)))
#define DI __device__ __forceinline__

constexpr int S = 2048, D = 640, F = 1728;
constexpr float EPS = 1e-5f;

constexpr u32 WS_URT = 0;
constexpr u32 WS_XT  = WS_URT + D * S * 2;
constexpr u32 WS_HT  = WS_XT + D * S * 2;
constexpr u32 WS_ST1 = WS_HT + D * S * 2;
constexpr u32 WS_R2  = WS_ST1 + S * 4;
constexpr u32 WS_CNT = WS_R2 + S * 4;
constexpr u32 WS_W1T = WS_CNT + 16384;
constexpr u32 WS_W2T = WS_W1T + 16 * 20 * 14 * 1024;
constexpr u32 WS_HM  = WS_W2T + 8 * 54 * 5 * 1024;
constexpr u32 WS_END = WS_HM + 128 * 54 * 1024;

typedef __amdgpu_buffer_rsrc_t rsrc_t;
DI float blo(u32 u) { return __uint_as_float(u << 16); }
DI float bhi(u32 u) { return __uint_as_float(u & 0xffff0000u); }
DI u32 pk2(float a, float b) { bf16x2 r; r[0] = (__bf16)a; r[1] = (__bf16)b; return __builtin_bit_cast(u32, r); }
DI float rbf(float a) { return blo(pk2(a, a)); }
DI f32x4 mfma16(u32x4 a, u32x4 b, f32x4 c) {
  return __builtin_amdgcn_mfma_f32_16x16x32_bf16(__builtin_bit_cast(s16x8, a), __builtin_bit_cast(s16x8, b), c, 0, 0, 0);
}
DI rsrc_t mkrsrc(const void* p, u32 bytes) { return __builtin_amdgcn_make_buffer_rsrc((void*)p, 0, bytes, 0x00020000); }
constexpr int AUX_WT = 16, AUX_NT = 2;
DI u32x4 ld128nt(rsrc_t rs, u32 off) { return __builtin_amdgcn_raw_buffer_load_b128(rs, off, 0, AUX_NT); }
DI u32x4 ld128(rsrc_t rs, u32 off) { return __builtin_amdgcn_raw_buffer_load_b128(rs, off, 0, 0); }
DI u32x2 ld64(rsrc_t rs, u32 off) { return __builtin_amdgcn_raw_buffer_load_b64(rs, off, 0, 0); }
template <int AUX = AUX_WT> DI void st128(rsrc_t rs, u32 off, u32x4 v) { __builtin_amdgcn_raw_buffer_store_b128(v, rs, off, 0, AUX); }
template <int AUX = AUX_WT> DI void st64(rsrc_t rs, u32 off, u32x2 v) { __builtin_amdgcn_raw_buffer_store_b64(v, rs, off, 0, AUX); }
constexpr int P_HT = 0, P_K0 = 0, P_W = 16;
DI void pin() { __builtin_amdgcn_sched_barrier(0); }
DI u32x2 trd(const char* p) { return __builtin_bit_cast(u32x2, __builtin_amdgcn_ds_read_tr16_b64_v4i16((LAS s16x4*)p)); }
DI u32x4 tr_frag(const char* p0, const char* p1) { const u32x2 a = trd(p0), b = trd(p1); return u32x4{a[0], a[1], b[0], b[1]}; }

#define STAMP_DECL
#define STAMP_PARAM
#define STAMP_PASS
#define ST(i) do {} while (0)
#define STAMP_OUT(K) do {} while (0)

extern "C" __global__ __launch_bounds__(256) void k0_prep(const u16* __restrict__ x, const u16* __restrict__ k, const u16* __restrict__ v, const u16* __restrict__ shk, char* __restrict__ ws) {
  __shared__ __attribute__((aligned(16))) u32 sm[2 * 64 * 32];
  const int b = blockIdx.x, tid = threadIdx.x;
  STAMP_DECL ST(0);
  asm volatile("" :: "s"(x), "s"(k), "s"(v), "s"(shk), "s"(ws));
  const rsrc_t rx = mkrsrc(x, S * D * 2);
  if (__builtin_expect(b >= 320, 0)) {
  if (b == 447 && tid < 16) ((u32*)(ws + WS_CNT))[tid * 32] = 0u;
  if (b == 446) {
    unsigned long long c0, c1;
    asm volatile("s_getpc_b64 s[40:41]\n\ts_add_u32 s40, s40, k1_conv@rel32@lo+4\n\ts_addc_u32 s41, s41, k1_conv@rel32@hi+12\n\ts_mov_b64 %0, s[40:41]\n\t"
                 "s_getpc_b64 s[40:41]\n\ts_add_u32 s40, s40, k9_end@rel32@lo+4\n\ts_addc_u32 s41, s41, k9_end@rel32@hi+12\n\ts_mov_b64 %1, s[40:41]"
                 : "=s"(c0), "=s"(c1) :: "s40", "s41", "scc");
    const rsrc_t rc = mkrsrc((const void*)c0, (u32)(c1 - c0));
    const u32 p0 = __builtin_amdgcn_raw_buffer_load_b32(rc, tid * 128, 0, 0), p1 = __builtin_amdgcn_raw_buffer_load_b32(rc, (tid + 256) * 128, 0, 0);
    unsigned long long kd;
    asm volatile("s_getpc_b64 s[40:41]\n\ts_add_u32 s40, s40, k0_prep.kd@rel32@lo+4\n\ts_addc_u32 s41, s41, k0_prep.kd@rel32@hi+12\n\ts_mov_b64 %0, s[40:41]" : "=s"(kd) :: "s40", "s41", "scc");
    const u32 p2 = __builtin_amdgcn_raw_buffer_load_b32(mkrsrc((const void*)(kd & ~127ull), 512), (tid & 3) * 128, 0, 0);
    const unsigned long long ka = (unsigned long long)__builtin_amdgcn_kernarg_segment_ptr() & ~4095ull;
    const u32 p3 = __builtin_amdgcn_raw_buffer_load_b32(mkrsrc((const void*)ka, 4096), (tid & 31) * 128, 0, 0);
    asm volatile("" :: "v"(p0), "v"(p1), "v"(p2), "v"(p3));
  }
    const int sb = b - 320, row = 64 * ((sb & 7) + 8 * (sb >> 5)) + 16 * ((sb >> 3) & 3) + (tid >> 4), s = tid & 15;
    u32x4 r[5];
#pragma unroll
    for (int i = 0; i < 5; ++i) r[i] = ld128nt(rx, (row * D + 8 * (s + 16 * i)) * 2);
    pin();
    float sum = 0.f;
#pragma unroll
    for (int i = 0; i < 5; ++i)
#pragma unroll
      for (int j = 0; j < 4; ++j) sum += blo(r[i][j]) + bhi(r[i][j]);
#pragma unroll
    for (int o = 1; o < 16; o <<= 1) sum += __shfl_xor(sum, o);
    const float mean = sum * (1.f / D);
    float q = 0.f;
#pragma unroll
    for (int i = 0; i < 5; ++i)
#pragma unroll
      for (int j = 0; j < 4; ++j) { const float a = blo(r[i][j]) - mean, c2 = bhi(r[i][j]) - mean; q += a * a + c2 * c2; }
#pragma unroll
    for (int o = 1; o < 16; o <<= 1) q += __shfl_xor(q, o);
    const float sd = __builtin_amdgcn_sqrtf(rbf(rbf(q * (1.f / D)) + EPS));
    if (s == 0) ((u32*)(ws + WS_ST1))[row] = pk2(sd, mean);
      ST(2); STAMP_OUT(0);
    return;
  }
  {
    const rsrc_t rk = mkrsrc(k, S * D * 2), rv = mkrsrc(v, S * D * 2), rw = mkrsrc(shk, D * 8);
    const int tb = (b & 7) + 8 * ((b >> 3) / 10), cb = (b >> 3) % 10, c = tid & 7, rg = tid >> 3;
    const int t = 64 * tb + 2 * rg, ch0 = 64 * cb + 8 * c;
    const u32 o = (u32)(t * D + ch0) * 2u;
    u32x4 kr[5], vr[2], xr[2], w[4];
#pragma unroll
    for (int i = 0; i < 5; ++i) kr[i] = ld128nt(rk, o + (u32)((i - 3) * D * 2));
#pragma unroll
    for (int i = 0; i < 2; ++i) { vr[i] = ld128nt(rv, o + i * D * 2); xr[i] = ld128nt(rx, o + i * D * 2); }
#pragma unroll
    for (int i = 0; i < 4; ++i) w[i] = ld128(rw, ch0 * 8 + 16 * i);
    pin();
    u32* sU = sm; u32* sX = sm + 64 * 32;
    const int colU = 31 - rg, colX = rg;
    const int pU = 4 * ((colU >> 2) ^ c) + (colU & 3), pX = 4 * ((colX >> 2) ^ c) + (colX & 3);
#pragma unroll
    for (int j = 0; j < 8; ++j) {
      float kk[5];
#pragma unroll
      for (int i = 0; i < 5; ++i) kk[i] = (j & 1) ? bhi(kr[i][j >> 1]) : blo(kr[i][j >> 1]);
      const u32 wa = w[j >> 1][(j & 1) * 2], wb = w[j >> 1][(j & 1) * 2 + 1];
      const float w0 = blo(wa), w1 = bhi(wa), w2 = blo(wb), w3 = bhi(wb);
      const float s0 = kk[0] * w3 + kk[1] * w2 + kk[2] * w1 + kk[3] * w0;
      const float s1 = kk[1] * w3 + kk[2] * w2 + kk[3] * w1 + kk[4] * w0;
      const u32 sp = pk2(s0, s1);
      const float v0 = (j & 1) ? bhi(vr[0][j >> 1]) : blo(vr[0][j >> 1]);
      const float v1 = (j & 1) ? bhi(vr[1][j >> 1]) : blo(vr[1][j >> 1]);
      sU[(8 * c + j) * 32 + pU] = pk2(bhi(sp) * v1, blo(sp) * v0);
      sX[(8 * c + j) * 32 + pX] = (j & 1) ? __builtin_amdgcn_perm(xr[1][j >> 1], xr[0][j >> 1], 0x07060302u) : __builtin_amdgcn_perm(xr[1][j >> 1], xr[0][j >> 1], 0x05040100u);
    }
    ST(1);
    __syncthreads();
#pragma unroll
    for (int i = 0; i < 2; ++i) {
      const int piece = tid + 256 * i, ch = piece >> 3, g = piece & 7, p = ch * 32 + 4 * (g ^ (ch >> 3));
      st128<P_K0>(mkrsrc(ws, WS_END), WS_URT + ((64 * cb + ch) * S + (1984 - 64 * tb) + 8 * g) * 2, *(const u32x4*)(sU + p));
      st128<P_K0>(mkrsrc(ws, WS_END), WS_XT + ((64 * cb + ch) * S + 64 * tb + 8 * g) * 2, *(const u32x4*)(sX + p));
    }
  }
  ST(2); STAMP_OUT(0);
}

constexpr int NW1 = 112;
constexpr int NTASK2 = 540, TPW = 2, NW2 = (NTASK2 + 4 * TPW - 1) / (4 * TPW);
constexpr int KC_STRIDE = 4160;
constexpr int K1_U = 32 * 1024, K1_LDS = K1_U + 2304 * 2;

DI void w1_strip(int r, const u16* wg, const u16* wu, char* ws, char* sm) {
  const int p = 8 * (r >> 4) + (r & 7), s = 2 * p + ((r >> 3) & 1);
  if (s >= 108) return;
  const int tid = threadIdx.x, lane = tid & 63, w = __builtin_amdgcn_readfirstlane(tid >> 6);
  const int m = s / 54, col0 = 32 * (s % 54), c4 = tid & 3, kg = tid >> 2;
  const rsrc_t rw = mkrsrc(m ? wu : wg, D * F * 2), rs = mkrsrc(ws, WS_END);
  u32x4 rr[10];
#pragma unroll
  for (int i = 0; i < 10; ++i) rr[i] = ld128nt(rw, ((kg + 64 * i) * F + col0 + 8 * c4) * 2);
  pin();
  const int g = lane >> 4, qp = (lane & 15) >> 2, p4 = lane & 3, c2 = w & 1;
  const u32x4 ones = {0x3f803f80u, 0x3f803f80u, 0x3f803f80u, 0x3f803f80u};
  u32x4 fr[10];
  f32x4 cs = {0, 0, 0, 0};
#pragma unroll
  for (int hf = 0; hf < 2; ++hf) {
    if (hf) __syncthreads();
#pragma unroll
    for (int i = 0; i < 5; ++i) *(u32x4*)(sm + (kg + 64 * i) * 80 + 16 * c4) = rr[5 * hf + i];
    __syncthreads();
#pragma unroll
    for (int i = 0; i < 5; ++i) {
      const char* im = sm + (32 * ((w >> 1) + 2 * i) + 8 * g + qp) * 80 + 32 * c2 + 8 * p4;
      fr[5 * hf + i] = tr_frag(im, im + 4 * 80);
      cs = mfma16(ones, fr[5 * hf + i], cs);
    }
  }
  float* red = (float*)(sm + 320 * 80);
  if (lane < 16) red[w * 16 + lane] = cs[0];
  __syncthreads();
  const float mu = (red[w * 16 + (lane & 15)] + red[(w ^ 2) * 16 + (lane & 15)]) * (1.f / D);
  const int ft = (col0 >> 4) + c2;
  const int c = ft < 84 ? ft / 7 : 12 + (ft - 84) / 6, pair = ft < 84 ? ft % 7 : (ft - 84) % 6;
#pragma unroll
  for (int i = 0; i < 10; ++i) {
    const int ks = 10 * (i / 5) + (w >> 1) + 2 * (i % 5);
    const u32x4 a = fr[i];
    st128<P_W>(rs, WS_W1T + ((c * 20 + ks) * 14 + 2 * pair + m) * 1024 + lane * 16,
          u32x4{pk2(blo(a[0]) - mu, bhi(a[0]) - mu), pk2(blo(a[1]) - mu, bhi(a[1]) - mu), pk2(blo(a[2]) - mu, bhi(a[2]) - mu), pk2(blo(a[3]) - mu, bhi(a[3]) - mu)});
  }
}
DI void w2_tasks(int pb, const u16* wd, char* ws, char* sm) {
  const int tid = threadIdx.x, lane = tid & 63, w = __builtin_amdgcn_readfirstlane(tid >> 6);
  const rsrc_t rw = mkrsrc(wd, D * F * 2), rs = mkrsrc(ws, WS_END);
  char* im = sm + w * (32 * 144);
  u32x4 ld[TPW][4];
  int task[TPW];
#pragma unroll
  for (int u = 0; u < TPW; ++u) {
    int t = (pb * 4 + w) * TPW + u; if (t >= NTASK2) t = NTASK2 - 1;
    task[u] = t;
    const int ks = t / 10, dr = t % 10;
    int lo, hi;
    if (ks < 36) { lo = 7 * (ks / 3) + 2 * (ks % 3); hi = lo + 1; } else if (ks < 48) { lo = 84 + 2 * (ks - 36); hi = lo + 1; } else { lo = 14 * (ks - 48) + 6; hi = lo + 7; }
#pragma unroll
    for (int i = 0; i < 4; ++i) { const int r = (lane >> 3) + 8 * i; ld[u][i] = ld128nt(rw, ((16 * (r < 16 ? lo : hi) + (r & 15)) * D + 64 * dr + 8 * (lane & 7)) * 2); }
  }
  pin();
  const int g = lane >> 4, qp = (lane & 15) >> 2, p4 = lane & 3;
#pragma unroll
  for (int u = 0; u < TPW; ++u) {
#pragma unroll
    for (int i = 0; i < 4; ++i) *(u32x4*)(im + ((lane >> 3) + 8 * i) * 144 + 16 * (lane & 7)) = ld[u][i];
    const int ks = task[u] / 10;
#pragma unroll
    for (int c4 = 0; c4 < 4; ++c4) {
      const int dt = 4 * (task[u] % 10) + c4;
      const u32x4 fr = tr_frag(im + (4 * g + qp) * 144 + 32 * c4 + 8 * p4, im + (16 + 4 * g + qp) * 144 + 32 * c4 + 8 * p4);
      st128<P_W>(rs, WS_W2T + (((dt / 5) * 54 + ks) * 5 + dt % 5) * 1024 + lane * 16, fr);
    }
  }
}

extern "C" __global__ __launch_bounds__(256, 4) void k1_conv(const u16* __restrict__ ssmk, const u16* __restrict__ ssmD, const u16* __restrict__ wg, const u16* __restrict__ wu, const u16* __restrict__ wd, char* __restrict__ ws) {
  extern __shared__ __attribute__((aligned(16))) char sm[];
  const int b = blockIdx.x, tid = threadIdx.x;
  STAMP_DECL ST(0);
  asm volatile("" :: "s"(ssmk), "s"(ssmD), "s"(ws));
  if (__builtin_expect(b >= D, 0)) {
    if (b >= D + NW1) w2_tasks(b - D - NW1, wd, ws, sm); else w1_strip(b - D, wg, wu, ws, sm);
    ST(5); STAMP_OUT(1); return;
  }
  const int d = b, lane = tid & 63, w = __builtin_amdgcn_readfirstlane(tid >> 6);
  const int l15 = lane & 15, q = lane >> 4;
  const rsrc_t rk = mkrsrc(ssmk + d * S, S * 2), rs = mkrsrc(ws, WS_END);
  const u32x4 c0 = ld128nt(rk, 16 * tid), c1 = ld128nt(rk, 16 * tid + 16);
  const u32x4 uu = ld128(rs, WS_URT + d * (S * 2) + 16 * tid);
  u32x2 xv[2]; u32x4 st[2];
#pragma unroll
  for (int i = 0; i < 2; ++i) {
    const int t = 256 * (2 * w + i) + 16 * l15 + 4 * q;
    xv[i] = ld64(rs, WS_XT + (d * S + t) * 2);
    st[i] = ld128(rs, WS_ST1 + t * 4);
  }
  const float Dd = blo(ssmD[d]);
  pin();
  ST(1);
  auto build = [&](int m, const u32x4& a, const u32x4& bb) {
    const u32 ww[8] = {a[0], a[1], a[2], a[3], bb[0], bb[1], bb[2], bb[3]};
    u32x4 o0, o1, o2, o3;
#pragma unroll
    for (int i = 0; i < 4; ++i) {
      o0[i] = __builtin_amdgcn_alignbyte(ww[i + 1], ww[i], 2); o1[i] = ww[i + 1];
      o2[i] = __builtin_amdgcn_alignbyte(ww[i + 2], ww[i + 1], 2); o3[i] = ww[i + 2];
    }
    *(u32x4*)(sm + 0 * KC_STRIDE + 16 * m) = o0; *(u32x4*)(sm + 1 * KC_STRIDE + 16 * m) = o1;
    *(u32x4*)(sm + 2 * KC_STRIDE + 16 * m) = o2; *(u32x4*)(sm + 3 * KC_STRIDE + 16 * m) = o3;
  };
  const u32x4 zero4 = {0, 0, 0, 0};
  build(tid + 2, c0, c1);
  if (tid < 2) build(1 - tid, zero4, tid == 0 ? c0 : zero4);
  *(u32x4*)(sm + K1_U + 16 * tid) = uu;
  if (tid < 32) *(u32x4*)(sm + K1_U + 4096 + 16 * tid) = zero4;
  __syncthreads();
  ST(2);
  f32x4 acc[8];
#pragma unroll
  for (int j = 0; j < 8; ++j) acc[j] = f32x4{0, 0, 0, 0};
#pragma unroll
  for (int rr = 0; rr < 2; ++rr) {
    const int rho = 2 * w + rr;
    const char* pf = sm + (l15 & 3) * KC_STRIDE + 2 * (l15 & 12) + 16 * q + 64 * rho;
    const char* pg = sm + K1_U + 480 - 32 * l15 + 64 * rho + 16 * q;
    u32x4 Ff[8], Gf[8];
#pragma unroll
    for (int i = 0; i < 8; ++i) {
      Gf[i] = *(const u32x4*)(pg + 512 * (7 - i));
      const u32x2 a = *(const volatile LAS u32x2*)(pf + 512 * i), bb = *(const volatile LAS u32x2*)(pf + 512 * i + 8);
      Ff[i] = u32x4{a[0], a[1], bb[0], bb[1]};
    }
#pragma unroll
    for (int i = 0; i < 8; ++i)
#pragma unroll
      for (int e = 0; e + i < 8; ++e) acc[i + e] = mfma16(Ff[i], Gf[e], acc[i + e]);
  }
  ST(3);
  __syncthreads();
  f32x4 own[2];
  {
    char* p = sm + w * 1024 + lane * 16;
#pragma unroll
    for (int j = 0; j < 8; ++j) if ((j >> 1) != w) *(f32x4*)(p + j * 4096) = acc[j];
#pragma unroll
    for (int i = 0; i < 2; ++i) own[i] = w == 0 ? acc[i] : w == 1 ? acc[2 + i] : w == 2 ? acc[4 + i] : acc[6 + i];
  }
  __syncthreads();
  ST(4);
#pragma unroll
  for (int i = 0; i < 2; ++i) {
    const int j = 2 * w + i;
    f32x4 y = own[i];
#pragma unroll
    for (int s = 1; s < 4; ++s) y += *(const f32x4*)(sm + (j * 4 + ((w + s) & 3)) * 1024 + lane * 16);
    const int t = 256 * j + 16 * l15 + 4 * q;
    const u32x2 ur = *(const u32x2*)(sm + K1_U + 2 * (2044 - t));
    const float uv[4] = {bhi(ur[1]), blo(ur[1]), bhi(ur[0]), blo(ur[0])};
    const float xx[4] = {blo(xv[i][0]), bhi(xv[i][0]), blo(xv[i][1]), bhi(xv[i][1])};
    float h[4];
#pragma unroll
    for (int r = 0; r < 4; ++r) {
      const float ln = (xx[r] - bhi(st[i][r])) * __builtin_amdgcn_rcpf(blo(st[i][r]));
      h[r] = fmaf(ln, fmaf(uv[r], Dd, y[r]), xx[r]);
    }
    st64<P_HT>(rs, WS_HT + (d * S + t) * 2, u32x2{pk2(h[0], h[1]), pk2(h[2], h[3])});
  }
  ST(5); STAMP_OUT(1);
}

template <int N> DI void waitv() { asm volatile("s_waitcnt vmcnt(%0)" ::"n"(N) : "memory"); }
DI void dma16(u32x4 srd, u32 lds, u32 voff, u32 soff) {
  asm volatile("s_mov_b32 m0, %0\n\ts_nop 0\n\tbuffer_load_dwordx4 %1, %2, %3 offen lds" ::"s"(lds), "v"(voff), "s"(srd), "s"(soff) : "memory");
}
DI u32x4 mksrd(const void* p, u32 bytes) { const unsigned long long a = (unsigned long long)p; return u32x4{(u32)a, (u32)(a >> 32) & 0xffffu, bytes, 0x00020000u}; }
DI u32 ldsaddr(const char* p) { return (u32)(unsigned long long)(LAS const char*)p; }
DI void bar() { __builtin_amdgcn_s_barrier(); }

template <int CNT, int K> DI void wait_big(bool big) { if (big) waitv<CNT * K>(); else waitv<(CNT - 1) * K>(); }
template <int CNT, int K> DI void tail_waits(bool big) { wait_big<CNT, K>(big); bar(); if constexpr (K > 0) tail_waits<CNT, K - 1>(big); }
template <int CNT, int NS, int NST, int SLOT, int SPLIT = NS, int OFF0 = 0, int OFF1 = 0>
struct Loader {
  u32x4 srd; u32 lb; u32 voff[CNT], sstep[CNT], ldst[CNT]; bool big;
  static DI u32 slot_off(int j) { return (u32)(j * SLOT + (j < SPLIT ? OFF0 : OFF1)); }
  DI void issue(int st, int j) {
    const u32 slot = slot_off(j);
#pragma unroll
    for (int i = 0; i < CNT; ++i) if (i < CNT - 1 || big) dma16(srd, lb + slot + ldst[i], voff[i], st * sstep[i]);
  }
  template <int S0, int S1> DI void prologue() {
#pragma unroll
    for (int s = S0; s < S1; ++s) issue(s, s);
  }
  DI void run() {
    static_assert(CNT * (NS - 3) <= 63, "vmcnt");
    wait_big<CNT, NS - 3>(big);
    bar();
    issue(NS - 2, NS - 2);
    int j = NS - 1;
    for (int n = 0; n + NS - 1 < NST; ++n) {
      wait_big<CNT, NS - 3>(big);
      bar();
      issue(n + NS - 1, j);
      j = j == NS - 1 ? 0 : j + 1;
    }
    tail_waits<CNT, NS - 3>(big);
  }
};

constexpr int G1_NS = 6, G1_SLOT = 22 * 1024, G1_NST = 20;
#define G2NS 12
constexpr int G2_NS = G2NS, G2_SLOT = 10 * 1024, G2_RING = 2 * G1_SLOT, G2_NST = 27;
DI constexpr int g2_off(int j) { return j < 8 ? G2_RING + j * G2_SLOT : (j - 8) * G2_SLOT; }
#define G2HW 9
constexpr int G2_HW = G2HW;
constexpr int G_AUX = G1_NS * G1_SLOT, G_LDS = G_AUX + 11 * 1024;
static_assert(G2_RING + 8 * G2_SLOT <= G_AUX && (G2_NS - 8) * G2_SLOT <= G2_RING, "phase B ring lies in the slots that phase A has left");
typedef __attribute__((address_space(1))) u32 gu32;

DI void mlp_loader(char* sm, char* ws, int rt, int c, int rt3, int dg, int lw, int lane) {
  constexpr int CNTA = 6, CNTB = 3;
  const u32x4 srd = mksrd(ws, WS_END);
  const u32 lb = ldsaddr(sm);
  {
    Loader<CNTA, G1_NS, G1_NST, G1_SLOT> L;
    L.srd = srd; L.lb = lb; L.big = lw < 2;
    const int l15 = lane & 15, q = lane >> 4;
#pragma unroll
    for (int i = 0; i < CNTA; ++i) {
      const int p = lw + 4 * i;
      L.ldst[i] = p * 1024;
      if (p < 14) { L.voff[i] = WS_W1T + (c * 20 * 14 + p) * 1024 + lane * 16; L.sstep[i] = 14 * 1024; }
      else { const int kk = 4 * (p - 14) + q, f = (kk & 3) | (((kk >> 3) & 1) << 2);
             L.voff[i] = WS_HT + kk * (S * 2) + rt * 256 + 16 * (l15 ^ (f << 1)); L.sstep[i] = 32 * S * 2; }
    }
    L.template prologue<0, G1_NS - 2>();
    L.run();
  }
#pragma unroll
  for (int i = 0; i < 3; ++i) {
    const int p = lw + 4 * i;
    if (p < 10) dma16(srd, lb + G_AUX + p * 1024, WS_HT + (80 * dg + 8 * p + (lane >> 3)) * (S * 2) + rt3 * 128 + (lane & 7) * 16, 0);
  }
  Loader<CNTB, G2_NS, G2_NST, G2_SLOT, 8, G2_RING, -8 * G2_SLOT> L;
  L.srd = srd; L.lb = lb; L.big = lw < 2;
#pragma unroll
  for (int i = 0; i < CNTB; ++i) { const int p = lw + 4 * i; L.ldst[i] = p * 1024; L.voff[i] = WS_W2T + (dg * 54 * 5 + p) * 1024 + lane * 16; L.sstep[i] = 10 * 1024;
  }
  L.template prologue<0, (G2_NS - 2 < 8 ? G2_NS - 2 : 8)>();
  bar();
  L.template prologue<8, G2_NS - 2>();
  L.run();
  __syncthreads();
  __syncthreads();
}

template <int NT, int T0, bool STAT>
DI void g1_body(char* sm, char* ws, int rt, int c, int w, int lane) {
  const int tw = w & 3, l15 = lane & 15, q = lane >> 4;
  const int qp = l15 >> 2, p4 = lane & 3, msk = 2 * qp | ((q & 1) << 3);
  int hoff[2];
#pragma unroll
  for (int i = 0; i < 2; ++i) hoff[i] = 14336 + (8 * q + qp) * 256 + 16 * ((4 * tw + 2 * i + (p4 >> 1)) ^ msk) + 8 * (p4 & 1);
  const int woff = T0 * 1024 + lane * 16;
  u32x4 W[NT];
  u32x2 h0a, h0b, h1a, h1b;
  f32x4 acc[2][NT], accS[2], accG[2];
#pragma unroll
  for (int i = 0; i < 2; ++i) {
    accS[i] = f32x4{0, 0, 0, 0}; accG[i] = f32x4{0, 0, 0, 0};
#pragma unroll
    for (int m = 0; m < NT; ++m) acc[i][m] = f32x4{0, 0, 0, 0};
  }
  const u32x4 ones = {0x3f803f80u, 0x3f803f80u, 0x3f803f80u, 0x3f803f80u};
  auto half0 = [&](const char* slot) {
    const u32x4 h0 = {h0a[0], h0a[1], h0b[0], h0b[1]};
#pragma unroll
    for (int m = 0; m < NT; ++m) {
      acc[0][m] = mfma16(W[m], h0, acc[0][m]);
      if (m == 0) h1a = trd(slot + hoff[1]);
      if (m == 1) h1b = trd(slot + hoff[1] + 1024);
      pin();
    }
    if (STAT) { accS[0] = mfma16(ones, h0, accS[0]); accG[0] = mfma16(h0, h0, accG[0]); pin(); }
  };
  auto half1 = [&](const char* nslot, bool doread) {
    const u32x4 h1 = {h1a[0], h1a[1], h1b[0], h1b[1]};
#pragma unroll
    for (int m = 0; m < NT; ++m) {
      acc[1][m] = mfma16(W[m], h1, acc[1][m]);
      if (doread) {
        W[m] = *(const u32x4*)(nslot + woff + m * 1024);
        if (m == 0) h0a = trd(nslot + hoff[0]);
        if (m == 1) h0b = trd(nslot + hoff[0] + 1024);
      }
      pin();
    }
    if (STAT) { accS[1] = mfma16(ones, h1, accS[1]); accG[1] = mfma16(h1, h1, accG[1]); pin(); }
  };
  bar();
#pragma unroll
  for (int m = 0; m < NT; ++m) {
    W[m] = *(const u32x4*)(sm + woff + m * 1024);
    if (m == 0) h0a = trd(sm + hoff[0]);
    if (m == 1) h0b = trd(sm + hoff[0] + 1024);
  }
  u32 cur = 0;
  half0(sm);
  for (int n = 0; n < G1_NST - 1; ++n) {
    bar();
    cur = cur == (G1_NS - 1) * G1_SLOT ? 0u : cur + G1_SLOT;
    half1(sm + cur, true);
    half0(sm + cur);
  }
  half1(sm, false);
  const rsrc_t rs = mkrsrc(ws, WS_END);
  const int ksb = 3 * c;
#pragma unroll
  for (int i = 0; i < 2; ++i) {
    const u32 ttile = 8 * rt + 2 * tw + i;
    u32 o[NT / 2][2];
#pragma unroll
    for (int pr = 0; pr < NT / 2; ++pr) {
      const f32x4 g = acc[i][2 * pr], u = acc[i][2 * pr + 1];
      o[pr][0] = pk2(fmaxf(g[0], 0.f) * u[0], fmaxf(g[1], 0.f) * u[1]); o[pr][1] = pk2(fmaxf(g[2], 0.f) * u[2], fmaxf(g[3], 0.f) * u[3]);
    }
    if (NT == 8) {
      st128(rs, WS_HM + ((ttile * 54 + ksb) * 64 + lane) * 16, u32x4{o[0][0], o[0][1], o[1][0], o[1][1]});
      st128(rs, WS_HM + ((ttile * 54 + ksb + 1) * 64 + lane) * 16, u32x4{o[2 % (NT / 2)][0], o[2 % (NT / 2)][1], o[3 % (NT / 2)][0], o[3 % (NT / 2)][1]});
    } else {
      st128(rs, WS_HM + ((ttile * 54 + ksb + 2) * 64 + lane) * 16, u32x4{o[0][0], o[0][1], o[1][0], o[1][1]});
      if (NT == 6) st64(rs, WS_HM + ((ttile * 54 + 48 + (c >> 1)) * 64 + lane) * 16 + 8 * (c & 1), u32x2{o[2 % (NT / 2)][0], o[2 % (NT / 2)][1]});
    }
    if (STAT) {
      if (tw == c - 12 && q == (l15 >> 2)) {
        const int r = l15 & 3;
        const float s2 = r == 0 ? accG[i][0] : r == 1 ? accG[i][1] : r == 2 ? accG[i][2] : accG[i][3];
        const float mean = accS[i][0] * (1.f / D), var = fmaxf(s2 * (1.f / D) - mean * mean, 0.f);
        const float rinv = __builtin_amdgcn_rcpf(rbf(__builtin_amdgcn_sqrtf(rbf(rbf(var) + EPS))));
        __builtin_amdgcn_raw_buffer_store_b32(__float_as_uint(rinv * rinv), rs, WS_R2 + (16 * ttile + l15) * 4, 0, AUX_WT);
      }
    }
  }
}

DI void g2_body(char* sm, char* ws, u16* out, int rt3, int dg, int rot, int w, int lane STAMP_PARAM) {
  const int tt = w & 3, kh = w >> 2, l15 = lane & 15, q = lane >> 4;
  const rsrc_t rs = mkrsrc(ws, WS_END);
  const u32 hbase = WS_HM + ((4 * rt3 + tt) * 54 + kh) * 1024 + lane * 16;
  u32x4 H[G2_HW];
  auto hload = [&](int n) { const int sr = n;
    return __builtin_amdgcn_raw_buffer_load_b128(rs, hbase, sr * 2048, AUX_WT);
  };
#pragma unroll
  for (int j = 0; j < G2_HW; ++j) H[j] = hload(j);
  const float r2 = __uint_as_float(__builtin_amdgcn_raw_buffer_load_b32(rs, WS_R2 + (64 * rt3 + 16 * tt + l15) * 4, 0, AUX_WT));
  pin();
  const char* wb = sm + kh * 5 * 1024 + lane * 16;
  u32x4 Wa[5], Wb[5];
  f32x4 acc[5];
#pragma unroll
  for (int m = 0; m < 5; ++m) { acc[m] = f32x4{0, 0, 0, 0}; Wa[m] = *(const u32x4*)(wb + g2_off(0) + m * 1024); }
  auto step = [&](u32x4 (&Wc)[5], u32x4 (&Wn)[5], int n) {
    if (n + 1 < G2_NST) bar();
#pragma unroll
    for (int m = 0; m < 5; ++m) {
      if (n + 1 < G2_NST) Wn[m] = *(const u32x4*)(wb + g2_off((n + 1) % G2_NS) + m * 1024);
      acc[m] = mfma16(Wc[m], H[n % G2_HW], acc[m]);
      pin();
    }
    if (n + G2_HW < G2_NST) { H[n % G2_HW] = hload(n + G2_HW); pin(); }
  };
#pragma unroll
  for (int n = 0; n < G2_NST; ++n) { if (n & 1) step(Wb, Wa, n); else step(Wa, Wb, n);
  }
  ST(4);
  __syncthreads();
  if (kh == 1) {
#pragma unroll
    for (int m = 0; m < 5; ++m) *(f32x4*)(sm + (tt * 5 + m) * 1024 + lane * 16) = acc[m];
  }
  __syncthreads();
  if (kh == 0) {
    const u16* res = (const u16*)(sm + G_AUX);
#pragma unroll
    for (int m = 0; m < 5; ++m) {
      const f32x4 o = acc[m] + *(const f32x4*)(sm + (tt * 5 + m) * 1024 + lane * 16);
      float r[4];
#pragma unroll
      for (int j = 0; j < 4; ++j) r[j] = __uint_as_float((u32)res[(16 * m + 4 * q + j) * 64 + 16 * tt + l15] << 16) + rbf(o[j] * r2);
      *(u32x2*)(out + (64 * rt3 + 16 * tt + l15) * D + 80 * dg + 16 * m + 4 * q) = u32x2{pk2(r[0], r[1]), pk2(r[2], r[3])};
    }
  }
}

DI void mlp_warm(char* sm, char* ws, int dg, int i, int lane) {
  const u32x4 srd = mksrd(ws, WS_END);
  const u32 lb = ldsaddr(sm) + G_AUX + 10 * 1024;
  const u32 voff = WS_W2T + (dg >> 2) * (4 * 54 * 5 * 1024) + i * 34560 + lane * 16;
  bar();
  for (int n = 0; n < 17; ++n) { bar(); dma16(srd, lb, voff, n * 2048); dma16(srd, lb, voff, n * 2048 + 1024); }
  waitv<0>();
}
constexpr int MLP_THREADS = 768;
extern "C" __global__ __launch_bounds__(MLP_THREADS) void k2_mlp(char* __restrict__ ws, u16* __restrict__ out) {
  extern __shared__ __attribute__((aligned(16))) char sm[];
  const int b = blockIdx.x, x = b & 7, i = b >> 3, cq = x >> 1;
  const int rt = 8 * (x & 1) + (i & 7), c = 4 * cq + (i >> 3);
  const int rt3 = 2 * rt + (cq >> 1), dg = 4 * (cq & 1) + (c & 3);
  const int lane = threadIdx.x & 63, w = __builtin_amdgcn_readfirstlane(threadIdx.x >> 6);
#define ROTA 0
#define ROTB 0
  const int rotA = ROTA ? (5 * (c & 3) + (5 * (i & 7)) / 2) % G1_NST : 0, rotB = ROTB ? (7 * (dg & 3) + (27 * (i & 7)) / 8) % G2_NST : 0;
  if (__builtin_expect(w >= 8, 1)) {
    if (w == 12) { mlp_warm(sm, ws, dg, i, lane); return; }
    mlp_loader(sm, ws, rt, c, rt3, dg, w - 8, lane);
    return;
  }
  STAMP_DECL ST(0);
  if (w < 4) g1_body<8, 0, false>(sm, ws, rt, c, w, lane);
  else if (c < 12) g1_body<6, 8, false>(sm, ws, rt, c, w, lane);
  else g1_body<4, 8, true>(sm, ws, rt, c, w, lane);
  ST(1);
  asm volatile("s_waitcnt vmcnt(0)" ::: "memory");
  bar();
  ST(2);
  if (threadIdx.x == 0) {
    gu32* cnt = (gu32*)(ws + WS_CNT) + (rt3 >> 1) * 32;
    __hip_atomic_fetch_add((gu32*)(ws + WS_CNT) + rt * 32, 1u, __ATOMIC_RELAXED, __HIP_MEMORY_SCOPE_AGENT);
    for (u32 spins = 0; __hip_atomic_load(cnt, __ATOMIC_RELAXED, __HIP_MEMORY_SCOPE_AGENT) < 16u && spins < (1u << 20); ++spins) __builtin_amdgcn_s_sleep(1);
  }
  __builtin_amdgcn_fence(__ATOMIC_ACQUIRE, "wavefront");
  bar();
  ST(3);
  g2_body(sm, ws, out, rt3, dg, rotB, w, lane STAMP_PASS);
  ST(5); STAMP_OUT(2);
}

extern "C" __global__ void k9_end() {}

extern "C" void kernel_launch(void* const* d_in, const int* in_sizes, int n_in, void* d_out, int out_size, void* d_ws, size_t ws_size, hipStream_t stream) {
  const u16* q = (const u16*)d_in[0]; const u16* k = (const u16*)d_in[1]; const u16* v = (const u16*)d_in[2];
  const u16* shk = (const u16*)d_in[3]; const u16* ssmk = (const u16*)d_in[4]; const u16* ssmD = (const u16*)d_in[5];
  const u16* wg = (const u16*)d_in[7]; const u16* wu = (const u16*)d_in[8]; const u16* wd = (const u16*)d_in[9];
  char* ws = (char*)d_ws;
  (void)hipFuncSetAttribute((const void*)k2_mlp, hipFuncAttributeMaxDynamicSharedMemorySize, G_LDS);
  k0_prep<<<448, 256, 0, stream>>>(q, k, v, shk, ws);
  k1_conv<<<D + NW1 + NW2, 256, K1_LDS, stream>>>(ssmk, ssmD, wg, wu, wd, ws);
  k2_mlp<<<256, MLP_THREADS, G_LDS, stream>>>(ws, (u16*)d_out);
}
```

```cpp
#include <hip/hip_runtime.h>
#include <stdint.h>
#define NO_WARM 1

typedef unsigned short u16;
typedef unsigned int u32;
typedef u32 u32x2 __attribute__((ext_vector_type(2)));
typedef u32 u32x4 __attribute__((ext_vector_type(4)));
typedef short s16x4 __attribute__((ext_vector_type(4)));
typedef short s16x8 __attribute__((ext_vector_type(8)));
typedef float f32x2 __attribute__((ext_vector_type(2)));
typedef float f32x4 __attribute__((ext_vector_type(4)));
typedef __bf16 bf16x2 __attribute__((ext_vector_type(2)));
#define LAS __attribute__((address_space(3)))
#define DI __device__ __forceinline__

constexpr int S = 2048, D = 640, F = 1728;
constexpr float EPS = 1e-5f;

constexpr u32 WS_URT = 0;
constexpr u32 WS_XT  = WS_URT + D * S * 2;
constexpr u32 WS_HT  = WS_XT + D * S * 2;
constexpr u32 WS_ST1 = WS_HT + D * S * 2;
constexpr u32 WS_R2  = WS_ST1 + S * 4;
constexpr u32 WS_CNT = WS_R2 + S * 4;
constexpr u32 WS_W1T = WS_CNT + 16384;
constexpr u32 WS_W2T = WS_W1T + 16 * 20 * 14 * 1024;
constexpr u32 WS_HM  = WS_W2T + 8 * 54 * 5 * 1024;
constexpr u32 WS_END = WS_HM + 128 * 54 * 1024;

typedef __amdgpu_buffer_rsrc_t rsrc_t;
DI float blo(u32 u) { return __uint_as_float(u << 16); }
DI float bhi(u32 u) { return __uint_as_float(u & 0xffff0000u); }
DI u32 pk2(float a, float b) { bf16x2 r; r[0] = (__bf16)a; r[1] = (__bf16)b; return __builtin_bit_cast(u32, r); }
DI float rbf(float a) { return blo(pk2(a, a)); }
DI f32x4 mfma16(u32x4 a, u32x4 b, f32x4 c) {
  return __builtin_amdgcn_mfma_f32_16x16x32_bf16(__builtin_bit_cast(s16x8, a), __builtin_bit_cast(s16x8, b), c, 0, 0, 0);
}
DI rsrc_t mkrsrc(const void* p, u32 bytes) { return __builtin_amdgcn_make_buffer_rsrc((void*)p, 0, bytes, 0x00020000); }
constexpr int AUX_WT = 16, AUX_NT = 2;
DI u32x4 ld128nt(rsrc_t rs, u32 off) { return __builtin_amdgcn_raw_buffer_load_b128(rs, off, 0, AUX_NT); }
DI u32x4 ld128(rsrc_t rs, u32 off) { return __builtin_amdgcn_raw_buffer_load_b128(rs, off, 0, 0); }
DI u32x2 ld64(rsrc_t rs, u32 off) { return __builtin_amdgcn_raw_buffer_load_b64(rs, off, 0, 0); }
template <int AUX = AUX_WT> DI void st128(rsrc_t rs, u32 off, u32x4 v) { __builtin_amdgcn_raw_buffer_store_b128(v, rs, off, 0, AUX); }
template <int AUX = AUX_WT> DI void st64(rsrc_t rs, u32 off, u32x2 v) { __builtin_amdgcn_raw_buffer_store_b64(v, rs, off, 0, AUX); }
constexpr int P_HT = 0, P_K0 = 0, P_W = 16;
#define HM_AUX 16
DI void pin() { __builtin_amdgcn_sched_barrier(0); }
DI u32x2 trd(const char* p) { return __builtin_bit_cast(u32x2, __builtin_amdgcn_ds_read_tr16_b64_v4i16((LAS s16x4*)p)); }
DI u32x4 tr_frag(const char* p0, const char* p1) { const u32x2 a = trd(p0), b = trd(p1); return u32x4{a[0], a[1], b[0], b[1]}; }

#define STAMP_DECL
#define STAMP_PARAM
#define STAMP_PASS
#define ST(i) do {} while (0)
#define STAMP_OUT(K) do {} while (0)

extern "C" __global__ __launch_bounds__(256) void k0_prep(const u16* __restrict__ x, const u16* __restrict__ k, const u16* __restrict__ v, const u16* __restrict__ shk, char* __restrict__ ws) {
  __shared__ __attribute__((aligned(16))) u32 sm[2 * 64 * 32];
  const int b = blockIdx.x, tid = threadIdx.x;
  STAMP_DECL ST(0);
  asm volatile("" :: "s"(x), "s"(k), "s"(v), "s"(shk), "s"(ws));
  const rsrc_t rx = mkrsrc(x, S * D * 2);
  if (__builtin_expect(b >= 320, 0)) {
  if (b == 447 && tid < 16) ((u32*)(ws + WS_CNT))[tid * 32] = 0u;
  if (b == 446) {
    unsigned long long c0, c1;
    asm volatile("s_getpc_b64 s[40:41]\n\ts_add_u32 s40, s40, k1_conv@rel32@lo+4\n\ts_addc_u32 s41, s41, k1_conv@rel32@hi+12\n\ts_mov_b64 %0, s[40:41]\n\t"
                 "s_getpc_b64 s[40:41]\n\ts_add_u32 s40, s40, k9_end@rel32@lo+4\n\ts_addc_u32 s41, s41, k9_end@rel32@hi+12\n\ts_mov_b64 %1, s[40:41]"
                 : "=s"(c0), "=s"(c1) :: "s40", "s41", "scc");
    const rsrc_t rc = mkrsrc((const void*)c0, (u32)(c1 - c0));
    const u32 p0 = __builtin_amdgcn_raw_buffer_load_b32(rc, tid * 128, 0, 0), p1 = __builtin_amdgcn_raw_buffer_load_b32(rc, (tid + 256) * 128, 0, 0);
    unsigned long long kd;
    asm volatile("s_getpc_b64 s[40:41]\n\ts_add_u32 s40, s40, k0_prep.kd@rel32@lo+4\n\ts_addc_u32 s41, s41, k0_prep.kd@rel32@hi+12\n\ts_mov_b64 %0, s[40:41]" : "=s"(kd) :: "s40", "s41", "scc");
    const u32 p2 = __builtin_amdgcn_raw_buffer_load_b32(mkrsrc((const void*)(kd & ~127ull), 512), (tid & 3) * 128, 0, 0);
    const unsigned long long ka = (unsigned long long)__builtin_amdgcn_kernarg_segment_ptr() & ~4095ull;
    const u32 p3 = __builtin_amdgcn_raw_buffer_load_b32(mkrsrc((const void*)ka, 4096), (tid & 31) * 128, 0, 0);
    asm volatile("" :: "v"(p0), "v"(p1), "v"(p2), "v"(p3));
  }
    const int sb = b - 320, row = 64 * ((sb & 7) + 8 * (sb >> 5)) + 16 * ((sb >> 3) & 3) + (tid >> 4), s = tid & 15;
    u32x4 r[5];
#pragma unroll
    for (int i = 0; i < 5; ++i) r[i] = ld128nt(rx, (row * D + 8 * (s + 16 * i)) * 2);
    pin();
    float sum = 0.f;
#pragma unroll
    for (int i = 0; i < 5; ++i)
#pragma unroll
      for (int j = 0; j < 4; ++j) sum += blo(r[i][j]) + bhi(r[i][j]);
#pragma unroll
    for (int o = 1; o < 16; o <<= 1) sum += __shfl_xor(sum, o);
    const float mean = sum * (1.f / D);
    float q = 0.f;
#pragma unroll
    for (int i = 0; i < 5; ++i)
#pragma unroll
      for (int j = 0; j < 4; ++j) { const float a = blo(r[i][j]) - mean, c2 = bhi(r[i][j]) - mean; q += a * a + c2 * c2; }
#pragma unroll
    for (int o = 1; o < 16; o <<= 1) q += __shfl_xor(q, o);
    const float sd = __builtin_amdgcn_sqrtf(rbf(rbf(q * (1.f / D)) + EPS));
    if (s == 0) ((u32*)(ws + WS_ST1))[row] = pk2(sd, mean);
      ST(2); STAMP_OUT(0);
    return;
  }
  {
    const rsrc_t rk = mkrsrc(k, S * D * 2), rv = mkrsrc(v, S * D * 2), rw = mkrsrc(shk, D * 8);
    const int tb = (b & 7) + 8 * ((b >> 3) / 10), cb = (b >> 3) % 10, c = tid & 7, rg = tid >> 3;
    const int t = 64 * tb + 2 * rg, ch0 = 64 * cb + 8 * c;
    const u32 o = (u32)(t * D + ch0) * 2u;
    u32x4 kr[5], vr[2], xr[2], w[4];
#pragma unroll
    for (int i = 0; i < 5; ++i) kr[i] = ld128nt(rk, o + (u32)((i - 3) * D * 2));
#pragma unroll
    for (int i = 0; i < 2; ++i) { vr[i] = ld128nt(rv, o + i * D * 2); xr[i] = ld128nt(rx, o + i * D * 2); }
#pragma unroll
    for (int i = 0; i < 4; ++i) w[i] = ld128(rw, ch0 * 8 + 16 * i);
    pin();
    u32* sU = sm; u32* sX = sm + 64 * 32;
    const int colU = 31 - rg, colX = rg;
    const int pU = 4 * ((colU >> 2) ^ c) + (colU & 3), pX = 4 * ((colX >> 2) ^ c) + (colX & 3);
#pragma unroll
    for (int j = 0; j < 8; ++j) {
      float kk[5];
#pragma unroll
      for (int i = 0; i < 5; ++i) kk[i] = (j & 1) ? bhi(kr[i][j >> 1]) : blo(kr[i][j >> 1]);
      const u32 wa = w[j >> 1][(j & 1) * 2], wb = w[j >> 1][(j & 1) * 2 + 1];
      const float w0 = blo(wa), w1 = bhi(wa), w2 = blo(wb), w3 = bhi(wb);
      const float s0 = kk[0] * w3 + kk[1] * w2 + kk[2] * w1 + kk[3] * w0;
      const float s1 = kk[1] * w3 + kk[2] * w2 + kk[3] * w1 + kk[4] * w0;
      const u32 sp = pk2(s0, s1);
      const float v0 = (j & 1) ? bhi(vr[0][j >> 1]) : blo(vr[0][j >> 1]);
      const float v1 = (j & 1) ? bhi(vr[1][j >> 1]) : blo(vr[1][j >> 1]);
      sU[(8 * c + j) * 32 + pU] = pk2(bhi(sp) * v1, blo(sp) * v0);
      sX[(8 * c + j) * 32 + pX] = (j & 1) ? __builtin_amdgcn_perm(xr[1][j >> 1], xr[0][j >> 1], 0x07060302u) : __builtin_amdgcn_perm(xr[1][j >> 1], xr[0][j >> 1], 0x05040100u);
    }
    ST(1);
    __syncthreads();
#pragma unroll
    for (int i = 0; i < 2; ++i) {
      const int piece = tid + 256 * i, ch = piece >> 3, g = piece & 7, p = ch * 32 + 4 * (g ^ (ch >> 3));
      st128<P_K0>(mkrsrc(ws, WS_END), WS_URT + ((64 * cb + ch) * S + (1984 - 64 * tb) + 8 * g) * 2, *(const u32x4*)(sU + p));
      st128<P_K0>(mkrsrc(ws, WS_END), WS_XT + ((64 * cb + ch) * S + 64 * tb + 8 * g) * 2, *(const u32x4*)(sX + p));
    }
  }
  ST(2); STAMP_OUT(0);
}

constexpr int NW1 = 112;
constexpr int NTASK2 = 540, TPW = 2, NW2 = (NTASK2 + 4 * TPW - 1) / (4 * TPW);
constexpr int KC_STRIDE = 4160;
constexpr int K1_U = 32 * 1024, K1_LDS = K1_U + 2304 * 2;

DI void w1_strip(int r, const u16* wg, const u16* wu, char* ws, char* sm) {
  const int p = 8 * (r >> 4) + (r & 7), s = 2 * p + ((r >> 3) & 1);
  if (s >= 108) return;
  const int tid = threadIdx.x, lane = tid & 63, w = __builtin_amdgcn_readfirstlane(tid >> 6);
  const int m = s / 54, col0 = 32 * (s % 54), c4 = tid & 3, kg = tid >> 2;
  const rsrc_t rw = mkrsrc(m ? wu : wg, D * F * 2), rs = mkrsrc(ws, WS_END);
  u32x4 rr[10];
#pragma unroll
  for (int i = 0; i < 10; ++i) rr[i] = ld128nt(rw, ((kg + 64 * i) * F + col0 + 8 * c4) * 2);
  pin();
  const int g = lane >> 4, qp = (lane & 15) >> 2, p4 = lane & 3, c2 = w & 1;
  const u32x4 ones = {0x3f803f80u, 0x3f803f80u, 0x3f803f80u, 0x3f803f80u};
  u32x4 fr[10];
  f32x4 cs = {0, 0, 0, 0};
#pragma unroll
  for (int hf = 0; hf < 2; ++hf) {
    if (hf) __syncthreads();
#pragma unroll
    for (int i = 0; i < 5; ++i) *(u32x4*)(sm + (kg + 64 * i) * 80 + 16 * c4) = rr[5 * hf + i];
    __syncthreads();
#pragma unroll
    for (int i = 0; i < 5; ++i) {
      const char* im = sm + (32 * ((w >> 1) + 2 * i) + 8 * g + qp) * 80 + 32 * c2 + 8 * p4;
      fr[5 * hf + i] = tr_frag(im, im + 4 * 80);
      cs = mfma16(ones, fr[5 * hf + i], cs);
    }
  }
  float* red = (float*)(sm + 320 * 80);
  if (lane < 16) red[w * 16 + lane] = cs[0];
  __syncthreads();
  const float mu = (red[w * 16 + (lane & 15)] + red[(w ^ 2) * 16 + (lane & 15)]) * (1.f / D);
  const int ft = (col0 >> 4) + c2;
  const int c = ft < 84 ? ft / 7 : 12 + (ft - 84) / 6, pair = ft < 84 ? ft % 7 : (ft - 84) % 6;
#pragma unroll
  for (int i = 0; i < 10; ++i) {
    const int ks = 10 * (i / 5) + (w >> 1) + 2 * (i % 5);
    const u32x4 a = fr[i];
    st128<P_W>(rs, WS_W1T + ((c * 20 + ks) * 14 + 2 * pair + m) * 1024 + lane * 16,
          u32x4{pk2(blo(a[0]) - mu, bhi(a[0]) - mu), pk2(blo(a[1]) - mu, bhi(a[1]) - mu), pk2(blo(a[2]) - mu, bhi(a[2]) - mu), pk2(blo(a[3]) - mu, bhi(a[3]) - mu)});
  }
}
DI void w2_tasks(int pb, const u16* wd, char* ws, char* sm) {
  const int tid = threadIdx.x, lane = tid & 63, w = __builtin_amdgcn_readfirstlane(tid >> 6);
  const rsrc_t rw = mkrsrc(wd, D * F * 2), rs = mkrsrc(ws, WS_END);
  char* im = sm + w * (32 * 144);
  u32x4 ld[TPW][4];
  int task[TPW];
#pragma unroll
  for (int u = 0; u < TPW; ++u) {
    int t = (pb * 4 + w) * TPW + u; if (t >= NTASK2) t = NTASK2 - 1;
    task[u] = t;
    const int ks = t / 10, dr = t % 10;
    int lo, hi;
    if (ks < 36) { lo = 7 * (ks / 3) + 2 * (ks % 3); hi = lo + 1; } else if (ks < 48) { lo = 84 + 2 * (ks - 36); hi = lo + 1; } else { lo = 14 * (ks - 48) + 6; hi = lo + 7; }
#pragma unroll
    for (int i = 0; i < 4; ++i) { const int r = (lane >> 3) + 8 * i; ld[u][i] = ld128nt(rw, ((16 * (r < 16 ? lo : hi) + (r & 15)) * D + 64 * dr + 8 * (lane & 7)) * 2); }
  }
  pin();
  const int g = lane >> 4, qp = (lane & 15) >> 2, p4 = lane & 3;
#pragma unroll
  for (int u = 0; u < TPW; ++u) {
#pragma unroll
    for (int i = 0; i < 4; ++i) *(u32x4*)(im + ((lane >> 3) + 8 * i) * 144 + 16 * (lane & 7)) = ld[u][i];
    const int ks = task[u] / 10;
#pragma unroll
    for (int c4 = 0; c4 < 4; ++c4) {
      const int dt = 4 * (task[u] % 10) + c4;
      const u32x4 fr = tr_frag(im + (4 * g + qp) * 144 + 32 * c4 + 8 * p4, im + (16 + 4 * g + qp) * 144 + 32 * c4 + 8 * p4);
      st128<P_W>(rs, WS_W2T + (((dt / 5) * 54 + ks) * 5 + dt % 5) * 1024 + lane * 16, fr);
    }
  }
}

extern "C" __global__ __launch_bounds__(256, 4) void k1_conv(const u16* __restrict__ ssmk, const u16* __restrict__ ssmD, const u16* __restrict__ wg, const u16* __restrict__ wu, const u16* __restrict__ wd, char* __restrict__ ws) {
  extern __shared__ __attribute__((aligned(16))) char sm[];
  const int b = blockIdx.x, tid = threadIdx.x;
  STAMP_DECL ST(0);
  asm volatile("" :: "s"(ssmk), "s"(ssmD), "s"(ws));
  if (__builtin_expect(b >= D, 0)) {
    if (b >= D + NW1) w2_tasks(b - D - NW1, wd, ws, sm); else w1_strip(b - D, wg, wu, ws, sm);
    ST(5); STAMP_OUT(1); return;
  }
  const int d = b, lane = tid & 63, w = __builtin_amdgcn_readfirstlane(tid >> 6);
  const int l15 = lane & 15, q = lane >> 4;
  const rsrc_t rk = mkrsrc(ssmk + d * S, S * 2), rs = mkrsrc(ws, WS_END);
  const u32x4 c0 = ld128nt(rk, 16 * tid), c1 = ld128nt(rk, 16 * tid + 16);
  const u32x4 uu = ld128(rs, WS_URT + d * (S * 2) + 16 * tid);
  u32x2 xv[2]; u32x4 st[2];
#pragma unroll
  for (int i = 0; i < 2; ++i) {
    const int t = 256 * (2 * w + i) + 16 * l15 + 4 * q;
    xv[i] = ld64(rs, WS_XT + (d * S + t) * 2);
    st[i] = ld128(rs, WS_ST1 + t * 4);
  }
  const float Dd = blo(ssmD[d]);
  pin();
  ST(1);
  auto build = [&](int m, const u32x4& a, const u32x4& bb) {
    const u32 ww[8] = {a[0], a[1], a[2], a[3], bb[0], bb[1], bb[2], bb[3]};
    u32x4 o0, o1, o2, o3;
#pragma unroll
    for (int i = 0; i < 4; ++i) {
      o0[i] = __builtin_amdgcn_alignbyte(ww[i + 1], ww[i], 2); o1[i] = ww[i + 1];
      o2[i] = __builtin_amdgcn_alignbyte(ww[i + 2], ww[i + 1], 2); o3[i] = ww[i + 2];
    }
    *(u32x4*)(sm + 0 * KC_STRIDE + 16 * m) = o0; *(u32x4*)(sm + 1 * KC_STRIDE + 16 * m) = o1;
    *(u32x4*)(sm + 2 * KC_STRIDE + 16 * m) = o2; *(u32x4*)(sm + 3 * KC_STRIDE + 16 * m) = o3;
  };
  const u32x4 zero4 = {0, 0, 0, 0};
  build(tid + 2, c0, c1);
  if (tid < 2) build(1 - tid, zero4, tid == 0 ? c0 : zero4);
  *(u32x4*)(sm + K1_U + 16 * tid) = uu;
  if (tid < 32) *(u32x4*)(sm + K1_U + 4096 + 16 * tid) = zero4;
  __syncthreads();
  ST(2);
  f32x4 acc[8];
#pragma unroll
  for (int j = 0; j < 8; ++j) acc[j] = f32x4{0, 0, 0, 0};
#pragma unroll
  for (int rr = 0; rr < 2; ++rr) {
    const int rho = 2 * w + rr;
    const char* pf = sm + (l15 & 3) * KC_STRIDE + 2 * (l15 & 12) + 16 * q + 64 * rho;
    const char* pg = sm + K1_U + 480 - 32 * l15 + 64 * rho + 16 * q;
    u32x4 Ff[8], Gf[8];
#pragma unroll
    for (int i = 0; i < 8; ++i) {
      Gf[i] = *(const u32x4*)(pg + 512 * (7 - i));
      const u32x2 a = *(const volatile LAS u32x2*)(pf + 512 * i), bb = *(const volatile LAS u32x2*)(pf + 512 * i + 8);
      Ff[i] = u32x4{a[0], a[1], bb[0], bb[1]};
    }
#pragma unroll
    for (int i = 0; i < 8; ++i)
#pragma unroll
      for (int e = 0; e + i < 8; ++e) acc[i + e] = mfma16(Ff[i], Gf[e], acc[i + e]);
  }
  ST(3);
  __syncthreads();
  f32x4 own[2];
  {
    char* p = sm + w * 1024 + lane * 16;
#pragma unroll
    for (int j = 0; j < 8; ++j) if ((j >> 1) != w) *(f32x4*)(p + j * 4096) = acc[j];
#pragma unroll
    for (int i = 0; i < 2; ++i) own[i] = w == 0 ? acc[i] : w == 1 ? acc[2 + i] : w == 2 ? acc[4 + i] : acc[6 + i];
  }
  __syncthreads();
  ST(4);
#pragma unroll
  for (int i = 0; i < 2; ++i) {
    const int j = 2 * w + i;
    f32x4 y = own[i];
#pragma unroll
    for (int s = 1; s < 4; ++s) y += *(const f32x4*)(sm + (j * 4 + ((w + s) & 3)) * 1024 + lane * 16);
    const int t = 256 * j + 16 * l15 + 4 * q;
    const u32x2 ur = *(const u32x2*)(sm + K1_U + 2 * (2044 - t));
    const float uv[4] = {bhi(ur[1]), blo(ur[1]), bhi(ur[0]), blo(ur[0])};
    const float xx[4] = {blo(xv[i][0]), bhi(xv[i][0]), blo(xv[i][1]), bhi(xv[i][1])};
    float h[4];
#pragma unroll
    for (int r = 0; r < 4; ++r) {
      const float ln = (xx[r] - bhi(st[i][r])) * __builtin_amdgcn_rcpf(blo(st[i][r]));
      h[r] = fmaf(ln, fmaf(uv[r], Dd, y[r]), xx[r]);
    }
    st64<P_HT>(rs, WS_HT + (d * S + t) * 2, u32x2{pk2(h[0], h[1]), pk2(h[2], h[3])});
  }
  ST(5); STAMP_OUT(1);
}

template <int N> DI void waitv() { asm volatile("s_waitcnt vmcnt(%0)" ::"n"(N) : "memory"); }
DI void dma16(u32x4 srd, u32 lds, u32 voff, u32 soff) {
  asm volatile("s_mov_b32 m0, %0\n\ts_nop 0\n\tbuffer_load_dwordx4 %1, %2, %3 offen lds" ::"s"(lds), "v"(voff), "s"(srd), "s"(soff) : "memory");
}
DI u32x4 mksrd(const void* p, u32 bytes) { const unsigned long long a = (unsigned long long)p; return u32x4{(u32)a, (u32)(a >> 32) & 0xffffu, bytes, 0x00020000u}; }
DI u32 ldsaddr(const char* p) { return (u32)(unsigned long long)(LAS const char*)p; }
DI void bar() { __builtin_amdgcn_s_barrier(); }

template <int CNT, int K> DI void wait_big(bool big) { if (big) waitv<CNT * K>(); else waitv<(CNT - 1) * K>(); }
template <int CNT, int K> DI void tail_waits(bool big) { wait_big<CNT, K>(big); bar(); if constexpr (K > 0) tail_waits<CNT, K - 1>(big); }
template <int CNT, int NS, int NST, class OFF>
struct Loader {
  u32x4 srd; u32 lb; u32 voff[CNT], sstep[CNT], ldst[CNT]; bool big;
  static DI u32 slot_off(int j) { return (u32)OFF::at(j); }
  DI void issue(int st, int j) {
    const u32 slot = slot_off(j);
#pragma unroll
    for (int i = 0; i < CNT; ++i) if (i < CNT - 1 || big) dma16(srd, lb + slot + ldst[i], voff[i], st * sstep[i]);
  }
  template <int S0, int S1> DI void prologue() {
#pragma unroll
    for (int s = S0; s < S1; ++s) issue(s, s);
  }
  DI void run() {
    static_assert(CNT * (NS - 3) <= 63, "vmcnt");
    wait_big<CNT, NS - 3>(big);
    bar();
    issue(NS - 2, NS - 2);
    int j = NS - 1;
    for (int n = 0; n + NS - 1 < NST; ++n) {
      wait_big<CNT, NS - 3>(big);
      bar();
      issue(n + NS - 1, j);
      j = j == NS - 1 ? 0 : j + 1;
    }
    tail_waits<CNT, NS - 3>(big);
  }
};

constexpr int G1_NS = 6, G1_SLOT = 22 * 1024, G1_NST = 20;
#define G2NS 14
constexpr int G2_NS = G2NS, G2_SLOT = 10 * 1024, G2_RING = 2 * G1_SLOT, G2_NST = 27, G2_PRE = 10;
DI constexpr int g2_off(int j) { return j < G2_PRE ? G2_RING + j * G2_SLOT : (j - G2_PRE) * G2_SLOT; }
struct OffA { static DI constexpr int at(int j) { return j * G1_SLOT; } };
struct OffB { static DI constexpr int at(int j) { return g2_off(j); } };
#define G2HW 9
constexpr int G2_HW = G2HW;
constexpr int G_AUX = G2_RING + G2_PRE * G2_SLOT, G_X0 = G2_RING + 8 * G2_SLOT, G_X1 = G_X0 + 8 * 1024, G_LDS = G_AUX + 10 * 1024;
static_assert(G_AUX >= G1_NS * G1_SLOT && G_LDS <= 160 * 1024, "");
static_assert((G2_NS - G2_PRE) * G2_SLOT <= G2_RING, "phase B ring lies in the slots that phase A has left");
typedef __attribute__((address_space(1))) u32 gu32;

DI void mlp_loader(char* sm, char* ws, int rt, int c, int rt3, int dg, int lw, int lane) {
  constexpr int CNTA = 6, CNTB = 3;
  const u32x4 srd = mksrd(ws, WS_END);
  const u32 lb = ldsaddr(sm);
  {
    Loader<CNTA, G1_NS, G1_NST, OffA> L;
    L.srd = srd; L.lb = lb; L.big = lw < 2;
    const int l15 = lane & 15, q = lane >> 4;
#pragma unroll
    for (int i = 0; i < CNTA; ++i) {
      const int p = lw + 4 * i;
      L.ldst[i] = p * 1024;
      if (p < 14) { L.voff[i] = WS_W1T + (c * 20 * 14 + p) * 1024 + lane * 16; L.sstep[i] = 14 * 1024; }
      else { const int kk = 4 * (p - 14) + q, f = (kk & 3) | (((kk >> 3) & 1) << 2);
             L.voff[i] = WS_HT + kk * (S * 2) + rt * 256 + 16 * (l15 ^ (f << 1)); L.sstep[i] = 32 * S * 2; }
    }
    L.template prologue<0, G1_NS - 2>();
    L.run();
  }
#pragma unroll
  for (int i = 0; i < 3; ++i) {
    const int p = lw + 4 * i;
    if (p < 10) dma16(srd, lb + G_AUX + p * 1024, WS_HT + (80 * dg + 8 * p + (lane >> 3)) * (S * 2) + rt3 * 128 + (lane & 7) * 16, 0);
  }
  Loader<CNTB, G2_NS, G2_NST, OffB> L;
  L.srd = srd; L.lb = lb; L.big = lw < 2;
#pragma unroll
  for (int i = 0; i < CNTB; ++i) { const int p = lw + 4 * i; L.ldst[i] = p * 1024; L.voff[i] = WS_W2T + (dg * 54 * 5 + p) * 1024 + lane * 16; L.sstep[i] = 10 * 1024;
  }
  L.template prologue<0, (G2_NS - 2 < G2_PRE ? G2_NS - 2 : G2_PRE)>();
  bar();
  L.template prologue<G2_PRE, G2_NS - 2>();
  L.run();
  __syncthreads();
}

template <int NT, int T0, bool STAT>
DI void g1_body(char* sm, char* ws, int rt, int c, int w, int lane) {
  const int tw = w & 3, l15 = lane & 15, q = lane >> 4;
  const int qp = l15 >> 2, p4 = lane & 3, msk = 2 * qp | ((q & 1) << 3);
  int hoff[2];
#pragma unroll
  for (int i = 0; i < 2; ++i) hoff[i] = 14336 + (8 * q + qp) * 256 + 16 * ((4 * tw + 2 * i + (p4 >> 1)) ^ msk) + 8 * (p4 & 1);
  const int woff = T0 * 1024 + lane * 16;
  u32x4 W[NT];
  u32x2 h0a, h0b, h1a, h1b;
  f32x4 acc[2][NT], accS[2], accG[2];
#pragma unroll
  for (int i = 0; i < 2; ++i) {
    accS[i] = f32x4{0, 0, 0, 0}; accG[i] = f32x4{0, 0, 0, 0};
#pragma unroll
    for (int m = 0; m < NT; ++m) acc[i][m] = f32x4{0, 0, 0, 0};
  }
  const u32x4 ones = {0x3f803f80u, 0x3f803f80u, 0x3f803f80u, 0x3f803f80u};
  auto half0 = [&](const char* slot) {
    const u32x4 h0 = {h0a[0], h0a[1], h0b[0], h0b[1]};
#pragma unroll
    for (int m = 0; m < NT; ++m) {
      acc[0][m] = mfma16(W[m], h0, acc[0][m]);
      if (m == 0) h1a = trd(slot + hoff[1]);
      if (m == 1) h1b = trd(slot + hoff[1] + 1024);
      pin();
    }
    if (STAT) { accS[0] = mfma16(ones, h0, accS[0]); accG[0] = mfma16(h0, h0, accG[0]); pin(); }
  };
  auto half1 = [&](const char* nslot, bool doread) {
    const u32x4 h1 = {h1a[0], h1a[1], h1b[0], h1b[1]};
#pragma unroll
    for (int m = 0; m < NT; ++m) {
      acc[1][m] = mfma16(W[m], h1, acc[1][m]);
      if (doread) {
        W[m] = *(const u32x4*)(nslot + woff + m * 1024);
        if (m == 0) h0a = trd(nslot + hoff[0]);
        if (m == 1) h0b = trd(nslot + hoff[0] + 1024);
      }
      pin();
    }
    if (STAT) { accS[1] = mfma16(ones, h1, accS[1]); accG[1] = mfma16(h1, h1, accG[1]); pin(); }
  };
  bar();
#pragma unroll
  for (int m = 0; m < NT; ++m) {
    W[m] = *(const u32x4*)(sm + woff + m * 1024);
    if (m == 0) h0a = trd(sm + hoff[0]);
    if (m == 1) h0b = trd(sm + hoff[0] + 1024);
  }
  u32 cur = 0;
  half0(sm);
  for (int n = 0; n < G1_NST - 1; ++n) {
    bar();
    cur = cur == (G1_NS - 1) * G1_SLOT ? 0u : cur + G1_SLOT;
    half1(sm + cur, true);
    half0(sm + cur);
  }
  half1(sm, false);
  const rsrc_t rs = mkrsrc(ws, WS_END);
  const int ksb = 3 * c;
#pragma unroll
  for (int i = 0; i < 2; ++i) {
    const u32 ttile = 8 * rt + 2 * tw + i;
    u32 o[NT / 2][2];
#pragma unroll
    for (int pr = 0; pr < NT / 2; ++pr) {
      const f32x4 g = acc[i][2 * pr], u = acc[i][2 * pr + 1];
      o[pr][0] = pk2(fmaxf(g[0], 0.f) * u[0], fmaxf(g[1], 0.f) * u[1]); o[pr][1] = pk2(fmaxf(g[2], 0.f) * u[2], fmaxf(g[3], 0.f) * u[3]);
    }
    if (NT == 8) {
      st128<HM_AUX>(rs, WS_HM + ((ttile * 54 + ksb) * 64 + lane) * 16, u32x4{o[0][0], o[0][1], o[1][0], o[1][1]});
      st128<HM_AUX>(rs, WS_HM + ((ttile * 54 + ksb + 1) * 64 + lane) * 16, u32x4{o[2 % (NT / 2)][0], o[2 % (NT / 2)][1], o[3 % (NT / 2)][0], o[3 % (NT / 2)][1]});
    } else {
      st128<HM_AUX>(rs, WS_HM + ((ttile * 54 + ksb + 2) * 64 + lane) * 16, u32x4{o[0][0], o[0][1], o[1][0], o[1][1]});
      if (NT == 6) st64<HM_AUX>(rs, WS_HM + ((ttile * 54 + 48 + (c >> 1)) * 64 + lane) * 16 + 8 * (c & 1), u32x2{o[2 % (NT / 2)][0], o[2 % (NT / 2)][1]});
    }
    if (STAT) {
      if (tw == c - 12 && q == (l15 >> 2)) {
        const int r = l15 & 3;
        const float s2 = r == 0 ? accG[i][0] : r == 1 ? accG[i][1] : r == 2 ? accG[i][2] : accG[i][3];
        const float mean = accS[i][0] * (1.f / D), var = fmaxf(s2 * (1.f / D) - mean * mean, 0.f);
        const float rinv = __builtin_amdgcn_rcpf(rbf(__builtin_amdgcn_sqrtf(rbf(rbf(var) + EPS))));
        __builtin_amdgcn_raw_buffer_store_b32(__float_as_uint(rinv * rinv), rs, WS_R2 + (16 * ttile + l15) * 4, 0, AUX_WT);
      }
    }
  }
}

DI void g2_body(char* sm, char* ws, u16* out, int rt3, int dg, int rot, int w, int lane STAMP_PARAM) {
  const int tt = w & 3, kh = w >> 2, l15 = lane & 15, q = lane >> 4;
  const rsrc_t rs = mkrsrc(ws, WS_END);
  const u32 hbase = WS_HM + ((4 * rt3 + tt) * 54 + kh) * 1024 + lane * 16;
  u32x4 H[G2_HW];
  auto hload = [&](int n) { const int sr = n;
    return __builtin_amdgcn_raw_buffer_load_b128(rs, hbase, sr * 2048, AUX_WT);
  };
#pragma unroll
  for (int j = 0; j < G2_HW; ++j) H[j] = hload(j);
  const float r2 = __uint_as_float(__builtin_amdgcn_raw_buffer_load_b32(rs, WS_R2 + (64 * rt3 + 16 * tt + l15) * 4, 0, AUX_WT));
  pin();
  const char* wb = sm + kh * 5 * 1024 + lane * 16;
  u32x4 Wa[5], Wb[5];
  f32x4 acc[5];
#pragma unroll
  for (int m = 0; m < 5; ++m) { acc[m] = f32x4{0, 0, 0, 0}; Wa[m] = *(const u32x4*)(wb + g2_off(0) + m * 1024); }
  auto step = [&](u32x4 (&Wc)[5], u32x4 (&Wn)[5], int n) {
    if (n + 1 < G2_NST) bar();
#pragma unroll
    for (int m = 0; m < 5; ++m) {
      if (n + 1 < G2_NST) Wn[m] = *(const u32x4*)(wb + g2_off((n + 1) % G2_NS) + m * 1024);
      acc[m] = mfma16(Wc[m], H[n % G2_HW], acc[m]);
      pin();
    }
    if (n + G2_HW < G2_NST) { H[n % G2_HW] = hload(n + G2_HW); pin(); }
  };
#pragma unroll
  for (int n = 0; n < G2_NST; ++n) { if (n & 1) step(Wb, Wa, n); else step(Wa, Wb, n);
  }
  ST(4);
  char* e0 = sm + G_X0 + tt * 2048 + lane * 16;
  char* e1 = sm + G_X1 + tt * 3072 + lane * 16;
  if (kh == 0) { *(f32x4*)e0 = acc[3]; *(f32x4*)(e0 + 1024) = acc[4]; }
  else { *(f32x4*)e1 = acc[0]; *(f32x4*)(e1 + 1024) = acc[1]; *(f32x4*)(e1 + 2048) = acc[2]; }
  __syncthreads();
  const char* res = sm + G_AUX + (4 * q + (l15 >> 2)) * 128 + (16 * tt + 4 * (lane & 3)) * 2;
  auto fin = [&](int m, const f32x4& o) {
    const u32x2 h = trd(res + m * 16 * 128);
    const float r0 = blo(h[0]) + rbf(o[0] * r2), r1 = bhi(h[0]) + rbf(o[1] * r2), r2b = blo(h[1]) + rbf(o[2] * r2), r3 = bhi(h[1]) + rbf(o[3] * r2);
    *(u32x2*)(out + (64 * rt3 + 16 * tt + l15) * D + 80 * dg + 16 * m + 4 * q) = u32x2{pk2(r0, r1), pk2(r2b, r3)};
  };
  if (kh == 0) {
#pragma unroll
    for (int m = 0; m < 3; ++m) fin(m, acc[m] + *(const f32x4*)(e1 + m * 1024));
  } else {
#pragma unroll
    for (int m = 3; m < 5; ++m) fin(m, acc[m] + *(const f32x4*)(e0 + (m - 3) * 1024));
  }
}

DI void mlp_warm(char* sm, char* ws, int dg, int i, int lane) {
  const u32x4 srd = mksrd(ws, WS_END);
  const u32 lb = ldsaddr(sm) + G_AUX + 10 * 1024;
  const u32 voff = WS_W2T + (dg >> 2) * (4 * 54 * 5 * 1024) + i * 34560 + lane * 16;
  bar();
  for (int n = 0; n < 17; ++n) { bar(); dma16(srd, lb, voff, n * 2048); dma16(srd, lb, voff, n * 2048 + 1024); }
  waitv<0>();
}
constexpr int MLP_THREADS = 768;
extern "C" __global__ __launch_bounds__(MLP_THREADS) void k2_mlp(char* __restrict__ ws, u16* __restrict__ out) {
  extern __shared__ __attribute__((aligned(16))) char sm[];
  const int b = blockIdx.x, x = b & 7, i = b >> 3, cq = x >> 1;
  const int rt = 8 * (x & 1) + (i & 7), c = 4 * cq + (i >> 3);
  const int rt3 = 2 * rt + (cq >> 1), dg = 4 * (cq & 1) + (c & 3);
  const int lane = threadIdx.x & 63, w = __builtin_amdgcn_readfirstlane(threadIdx.x >> 6);
#define ROTA 0
#define ROTB 0
  const int rotA = ROTA ? (5 * (c & 3) + (5 * (i & 7)) / 2) % G1_NST : 0, rotB = ROTB ? (7 * (dg & 3) + (27 * (i & 7)) / 8) % G2_NST : 0;
  if (__builtin_expect(w >= 8, 1)) {
    if (w == 12) { mlp_warm(sm, ws, dg, i, lane); return; }
    mlp_loader(sm, ws, rt, c, rt3, dg, w - 8, lane);
    return;
  }
  STAMP_DECL ST(0);
  if (w < 4) g1_body<8, 0, false>(sm, ws, rt, c, w, lane);
  else if (c < 12) g1_body<6, 8, false>(sm, ws, rt, c, w, lane);
  else g1_body<4, 8, true>(sm, ws, rt, c, w, lane);
  ST(1);
  asm volatile("s_waitcnt vmcnt(0)" ::: "memory");
  bar();
  ST(2);
  if (threadIdx.x == 0) {
    gu32* cnt = (gu32*)(ws + WS_CNT) + (rt3 >> 1) * 32;
    __hip_atomic_fetch_add((gu32*)(ws + WS_CNT) + rt * 32, 1u, __ATOMIC_RELAXED, __HIP_MEMORY_SCOPE_AGENT);
    for (u32 spins = 0; __hip_atomic_load(cnt, __ATOMIC_RELAXED, __HIP_MEMORY_SCOPE_AGENT) < 16u && spins < (1u << 20); ++spins) __builtin_amdgcn_s_sleep(1);
  }
  __builtin_amdgcn_fence(__ATOMIC_ACQUIRE, "wavefront");
  bar();
  ST(3);
  g2_body(sm, ws, out, rt3, dg, rotB, w, lane STAMP_PASS);
  ST(5); STAMP_OUT(2);
}

extern "C" __global__ void k9_end() {}

extern "C" void kernel_launch(void* const* d_in, const int* in_sizes, int n_in, void* d_out, int out_size, void* d_ws, size_t ws_size, hipStream_t stream) {
  const u16* q = (const u16*)d_in[0]; const u16* k = (const u16*)d_in[1]; const u16* v = (const u16*)d_in[2];
  const u16* shk = (const u16*)d_in[3]; const u16* ssmk = (const u16*)d_in[4]; const u16* ssmD = (const u16*)d_in[5];
  const u16* wg = (const u16*)d_in[7]; const u16* wu = (const u16*)d_in[8]; const u16* wd = (const u16*)d_in[9];
  char* ws = (char*)d_ws;
  (void)hipFuncSetAttribute((const void*)k2_mlp, hipFuncAttributeMaxDynamicSharedMemorySize, G_LDS);
  k0_prep<<<448, 256, 0, stream>>>(q, k, v, shk, ws);
  k1_conv<<<D + NW1 + NW2, 256, K1_LDS, stream>>>(ssmk, ssmD, wg, wu, wd, ws);
  k2_mlp<<<256, MLP_THREADS, G_LDS, stream>>>(ws, (u16*)d_out);
}
```

```cpp
#include <hip/hip_runtime.h>
#include <stdint.h>

typedef unsigned short u16;
typedef unsigned int u32;
typedef u32 u32x2 __attribute__((ext_vector_type(2)));
typedef u32 u32x4 __attribute__((ext_vector_type(4)));
typedef short s16x4 __attribute__((ext_vector_type(4)));
typedef short s16x8 __attribute__((ext_vector_type(8)));
typedef float f32x2 __attribute__((ext_vector_type(2)));
typedef float f32x4 __attribute__((ext_vector_type(4)));
typedef __bf16 bf16x2 __attribute__((ext_vector_type(2)));
#define LAS __attribute__((address_space(3)))
#define DI __device__ __forceinline__

constexpr int S = 2048, D = 640, F = 1728;
constexpr float EPS = 1e-5f;

constexpr u32 WS_URT = 0;
constexpr u32 WS_XT  = WS_URT + D * S * 2;
constexpr u32 WS_HT  = WS_XT + D * S * 2;
constexpr u32 WS_ST1 = WS_HT + D * S * 2;
constexpr u32 WS_R2  = WS_ST1 + S * 4;
constexpr u32 WS_CNT = WS_R2 + S * 4;
constexpr u32 WS_W1T = WS_CNT + 16384;
constexpr u32 WS_W2T = WS_W1T + 16 * 20 * 14 * 1024;
constexpr u32 WS_HM  = WS_W2T + 8 * 54 * 5 * 1024;
constexpr u32 WS_END = WS_HM + 128 * 54 * 1024;

typedef __amdgpu_buffer_rsrc_t rsrc_t;
DI float blo(u32 u) { return __uint_as_float(u << 16); }
DI float bhi(u32 u) { return __uint_as_float(u & 0xffff0000u); }
DI u32 pk2(float a, float b) { bf16x2 r; r[0] = (__bf16)a; r[1] = (__bf16)b; return __builtin_bit_cast(u32, r); }
DI float rbf(float a) { return blo(pk2(a, a)); }
DI f32x4 mfma16(u32x4 a, u32x4 b, f32x4 c) {
  return __builtin_amdgcn_mfma_f32_16x16x32_bf16(__builtin_bit_cast(s16x8, a), __builtin_bit_cast(s16x8, b), c, 0, 0, 0);
}
DI rsrc_t mkrsrc(const void* p, u32 bytes) { return __builtin_amdgcn_make_buffer_rsrc((void*)p, 0, bytes, 0x00020000); }
constexpr int AUX_WT = 16, AUX_NT = 2;
DI u32x4 ld128nt(rsrc_t rs, u32 off) { return __builtin_amdgcn_raw_buffer_load_b128(rs, off, 0, AUX_NT); }
DI u32x4 ld128(rsrc_t rs, u32 off) { return __builtin_amdgcn_raw_buffer_load_b128(rs, off, 0, 0); }
DI u32x2 ld64(rsrc_t rs, u32 off) { return __builtin_amdgcn_raw_buffer_load_b64(rs, off, 0, 0); }
template <int AUX = AUX_WT> DI void st128(rsrc_t rs, u32 off, u32x4 v) { __builtin_amdgcn_raw_buffer_store_b128(v, rs, off, 0, AUX); }
template <int AUX = AUX_WT> DI void st64(rsrc_t rs, u32 off, u32x2 v) { __builtin_amdgcn_raw_buffer_store_b64(v, rs, off, 0, AUX); }
DI void pin() { __builtin_amdgcn_sched_barrier(0); }
DI u32x2 trd(const char* p) { return __builtin_bit_cast(u32x2, __builtin_amdgcn_ds_read_tr16_b64_v4i16((LAS s16x4*)p)); }
DI u32x4 tr_frag(const char* p0, const char* p1) { const u32x2 a = trd(p0), b = trd(p1); return u32x4{a[0], a[1], b[0], b[1]}; }


extern "C" __global__ __launch_bounds__(256) void k0_prep(const u16* __restrict__ x, const u16* __restrict__ k, const u16* __restrict__ v, const u16* __restrict__ shk, char* __restrict__ ws) {
  __shared__ __attribute__((aligned(16))) u32 sm[2 * 64 * 32];
  const int b = (int)blockIdx.x - 8, tid = threadIdx.x;
  asm volatile("" :: "s"(x), "s"(k), "s"(v), "s"(shk), "s"(ws));
  const rsrc_t rx = mkrsrc(x, S * D * 2);
  if (__builtin_expect((u32)b >= 320u, 0)) {
  if (b == -7 && tid < 16) ((u32*)(ws + WS_CNT))[tid * 32] = 0u;
  if (b == -8) {
    unsigned long long c0, c1;
    asm volatile("s_getpc_b64 s[40:41]\n\ts_add_u32 s40, s40, k1_conv@rel32@lo+4\n\ts_addc_u32 s41, s41, k1_conv@rel32@hi+12\n\ts_mov_b64 %0, s[40:41]\n\t"
                 "s_getpc_b64 s[40:41]\n\ts_add_u32 s40, s40, k9_end@rel32@lo+4\n\ts_addc_u32 s41, s41, k9_end@rel32@hi+12\n\ts_mov_b64 %1, s[40:41]"
                 : "=s"(c0), "=s"(c1) :: "s40", "s41", "scc");
    const rsrc_t rc = mkrsrc((const void*)c0, (u32)(c1 - c0));
    const u32 p0 = __builtin_amdgcn_raw_buffer_load_b32(rc, tid * 128, 0, 0), p1 = __builtin_amdgcn_raw_buffer_load_b32(rc, (tid + 256) * 128, 0, 0);
    unsigned long long kd;
    asm volatile("s_getpc_b64 s[40:41]\n\ts_add_u32 s40, s40, k0_prep.kd@rel32@lo+4\n\ts_addc_u32 s41, s41, k0_prep.kd@rel32@hi+12\n\ts_mov_b64 %0, s[40:41]" : "=s"(kd) :: "s40", "s41", "scc");
    const u32 p2 = __builtin_amdgcn_raw_buffer_load_b32(mkrsrc((const void*)(kd & ~127ull), 512), (tid & 3) * 128, 0, 0);
    const unsigned long long ka = (unsigned long long)__builtin_amdgcn_kernarg_segment_ptr() & ~4095ull;
    const u32 p3 = __builtin_amdgcn_raw_buffer_load_b32(mkrsrc((const void*)ka, 4096), (tid & 31) * 128, 0, 0);
    asm volatile("" :: "v"(p0), "v"(p1), "v"(p2), "v"(p3));
  }
    if (b < 0) return;
    const int sb = b - 320, row = 64 * ((sb & 7) + 8 * (sb >> 5)) + 16 * ((sb >> 3) & 3) + (tid >> 4), s = tid & 15;
    u32x4 r[5];
#pragma unroll
    for (int i = 0; i < 5; ++i) r[i] = ld128nt(rx, (row * D + 8 * (s + 16 * i)) * 2);
    pin();
    float sum = 0.f;
#pragma unroll
    for (int i = 0; i < 5; ++i)
#pragma unroll
      for (int j = 0; j < 4; ++j) sum += blo(r[i][j]) + bhi(r[i][j]);
#pragma unroll
    for (int o = 1; o < 16; o <<= 1) sum += __shfl_xor(sum, o);
    const float mean = sum * (1.f / D);
    float q = 0.f;
#pragma unroll
    for (int i = 0; i < 5; ++i)
#pragma unroll
      for (int j = 0; j < 4; ++j) { const float a = blo(r[i][j]) - mean, c2 = bhi(r[i][j]) - mean; q += a * a + c2 * c2; }
#pragma unroll
    for (int o = 1; o < 16; o <<= 1) q += __shfl_xor(q, o);
    const float sd = __builtin_amdgcn_sqrtf(rbf(rbf(q * (1.f / D)) + EPS));
    if (s == 0) ((u32*)(ws + WS_ST1))[row] = pk2(sd, mean);
    return;
  }
  {
    const rsrc_t rk = mkrsrc(k, S * D * 2), rv = mkrsrc(v, S * D * 2), rw = mkrsrc(shk, D * 8);
    const int tb = (b & 7) + 8 * ((b >> 3) / 10), cb = (b >> 3) % 10, c = tid & 7, rg = tid >> 3;
    const int t = 64 * tb + 2 * rg, ch0 = 64 * cb + 8 * c;
    const u32 o = (u32)(t * D + ch0) * 2u;
    u32x4 kr[5], vr[2], xr[2], w[4];
#pragma unroll
    for (int i = 0; i < 5; ++i) kr[i] = ld128nt(rk, o + (u32)((i - 3) * D * 2));
#pragma unroll
    for (int i = 0; i < 2; ++i) { vr[i] = ld128nt(rv, o + i * D * 2); xr[i] = ld128nt(rx, o + i * D * 2); }
#pragma unroll
    for (int i = 0; i < 4; ++i) w[i] = ld128(rw, ch0 * 8 + 16 * i);
    pin();
    u32* sU = sm; u32* sX = sm + 64 * 32;
    const int colU = 31 - rg, colX = rg;
    const int pU = 4 * ((colU >> 2) ^ c) + (colU & 3), pX = 4 * ((colX >> 2) ^ c) + (colX & 3);
#pragma unroll
    for (int j = 0; j < 8; ++j) {
      float kk[5];
#pragma unroll
      for (int i = 0; i < 5; ++i) kk[i] = (j & 1) ? bhi(kr[i][j >> 1]) : blo(kr[i][j >> 1]);
      const u32 wa = w[j >> 1][(j & 1) * 2], wb = w[j >> 1][(j & 1) * 2 + 1];
      const float w0 = blo(wa), w1 = bhi(wa), w2 = blo(wb), w3 = bhi(wb);
      const float s0 = kk[0] * w3 + kk[1] * w2 + kk[2] * w1 + kk[3] * w0;
      const float s1 = kk[1] * w3 + kk[2] * w2 + kk[3] * w1 + kk[4] * w0;
      const u32 sp = pk2(s0, s1);
      const float v0 = (j & 1) ? bhi(vr[0][j >> 1]) : blo(vr[0][j >> 1]);
      const float v1 = (j & 1) ? bhi(vr[1][j >> 1]) : blo(vr[1][j >> 1]);
      sU[(8 * c + j) * 32 + pU] = pk2(bhi(sp) * v1, blo(sp) * v0);
      sX[(8 * c + j) * 32 + pX] = (j & 1) ? __builtin_amdgcn_perm(xr[1][j >> 1], xr[0][j >> 1], 0x07060302u) : __builtin_amdgcn_perm(xr[1][j >> 1], xr[0][j >> 1], 0x05040100u);
    }
    __syncthreads();
#pragma unroll
    for (int i = 0; i < 2; ++i) {
      const int piece = tid + 256 * i, ch = piece >> 3, g = piece & 7, p = ch * 32 + 4 * (g ^ (ch >> 3));
      st128<0>(mkrsrc(ws, WS_END), WS_URT + ((64 * cb + ch) * S + (1984 - 64 * tb) + 8 * g) * 2, *(const u32x4*)(sU + p));
      st128<0>(mkrsrc(ws, WS_END), WS_XT + ((64 * cb + ch) * S + 64 * tb + 8 * g) * 2, *(const u32x4*)(sX + p));
    }
  }
}

constexpr int NW1 = 112;
constexpr int NTASK2 = 540, TPW = 2, NW2 = (NTASK2 + 4 * TPW - 1) / (4 * TPW);
constexpr int KC_STRIDE = 4160;
constexpr int K1_U = 32 * 1024, K1_LDS = K1_U + 2304 * 2;

DI void w1_strip(int r, const u16* wg, const u16* wu, char* ws, char* sm) {
  const int p = 8 * (r >> 4) + (r & 7), s = 2 * p + ((r >> 3) & 1);
  if (s >= 108) return;
  const int tid = threadIdx.x, lane = tid & 63, w = __builtin_amdgcn_readfirstlane(tid >> 6);
  const int m = s / 54, col0 = 32 * (s % 54), c4 = tid & 3, kg = tid >> 2;
  const rsrc_t rw = mkrsrc(m ? wu : wg, D * F * 2), rs = mkrsrc(ws, WS_END);
  u32x4 rr[10];
#pragma unroll
  for (int i = 0; i < 10; ++i) rr[i] = ld128nt(rw, ((kg + 64 * i) * F + col0 + 8 * c4) * 2);
  pin();
  const int g = lane >> 4, qp = (lane & 15) >> 2, p4 = lane & 3, c2 = w & 1;
  const u32x4 ones = {0x3f803f80u, 0x3f803f80u, 0x3f803f80u, 0x3f803f80u};
  u32x4 fr[10];
  f32x4 cs = {0, 0, 0, 0};
#pragma unroll
  for (int hf = 0; hf < 2; ++hf) {
    if (hf) __syncthreads();
#pragma unroll
    for (int i = 0; i < 5; ++i) *(u32x4*)(sm + (kg + 64 * i) * 80 + 16 * c4) = rr[5 * hf + i];
    __syncthreads();
#pragma unroll
    for (int i = 0; i < 5; ++i) {
      const char* im = sm + (32 * ((w >> 1) + 2 * i) + 8 * g + qp) * 80 + 32 * c2 + 8 * p4;
      fr[5 * hf + i] = tr_frag(im, im + 4 * 80);
      cs = mfma16(ones, fr[5 * hf + i], cs);
    }
  }
  float* red = (float*)(sm + 320 * 80);
  if (lane < 16) red[w * 16 + lane] = cs[0];
  __syncthreads();
  const float mu = (red[w * 16 + (lane & 15)] + red[(w ^ 2) * 16 + (lane & 15)]) * (1.f / D);
  const int ft = (col0 >> 4) + c2;
  const int c = ft < 84 ? ft / 7 : 12 + (ft - 84) / 6, pair = ft < 84 ? ft % 7 : (ft - 84) % 6;
#pragma unroll
  for (int i = 0; i < 10; ++i) {
    const int ks = 10 * (i / 5) + (w >> 1) + 2 * (i % 5);
    const u32x4 a = fr[i];
    st128(rs, WS_W1T + ((c * 20 + ks) * 14 + 2 * pair + m) * 1024 + lane * 16,
          u32x4{pk2(blo(a[0]) - mu, bhi(a[0]) - mu), pk2(blo(a[1]) - mu, bhi(a[1]) - mu), pk2(blo(a[2]) - mu, bhi(a[2]) - mu), pk2(blo(a[3]) - mu, bhi(a[3]) - mu)});
  }
}
DI void w2_tasks(int pb, const u16* wd, char* ws, char* sm) {
  const int tid = threadIdx.x, lane = tid & 63, w = __builtin_amdgcn_readfirstlane(tid >> 6);
  const rsrc_t rw = mkrsrc(wd, D * F * 2), rs = mkrsrc(ws, WS_END);
  char* im = sm + w * (32 * 144);
  u32x4 ld[TPW][4];
  int task[TPW];
#pragma unroll
  for (int u = 0; u < TPW; ++u) {
    int t = (pb * 4 + w) * TPW + u; if (t >= NTASK2) t = NTASK2 - 1;
    task[u] = t;
    const int ks = t / 10, dr = t % 10;
    int lo, hi;
    if (ks < 36) { lo = 7 * (ks / 3) + 2 * (ks % 3); hi = lo + 1; } else if (ks < 48) { lo = 84 + 2 * (ks - 36); hi = lo + 1; } else { lo = 14 * (ks - 48) + 6; hi = lo + 7; }
#pragma unroll
    for (int i = 0; i < 4; ++i) { const int r = (lane >> 3) + 8 * i; ld[u][i] = ld128nt(rw, ((16 * (r < 16 ? lo : hi) + (r & 15)) * D + 64 * dr + 8 * (lane & 7)) * 2); }
  }
  pin();
  const int g = lane >> 4, qp = (lane & 15) >> 2, p4 = lane & 3;
#pragma unroll
  for (int u = 0; u < TPW; ++u) {
#pragma unroll
    for (int i = 0; i < 4; ++i) *(u32x4*)(im + ((lane >> 3) + 8 * i) * 144 + 16 * (lane & 7)) = ld[u][i];
    const int ks = task[u] / 10;
#pragma unroll
    for (int c4 = 0; c4 < 4; ++c4) {
      const int dt = 4 * (task[u] % 10) + c4;
      const u32x4 fr = tr_frag(im + (4 * g + qp) * 144 + 32 * c4 + 8 * p4, im + (16 + 4 * g + qp) * 144 + 32 * c4 + 8 * p4);
      st128(rs, WS_W2T + (((dt / 5) * 54 + ks) * 5 + dt % 5) * 1024 + lane * 16, fr);
    }
  }
}

extern "C" __global__ __launch_bounds__(256, 4) void k1_conv(const u16* __restrict__ ssmk, const u16* __restrict__ ssmD, const u16* __restrict__ wg, const u16* __restrict__ wu, const u16* __restrict__ wd, char* __restrict__ ws) {
  extern __shared__ __attribute__((aligned(16))) char sm[];
  const int b = blockIdx.x, tid = threadIdx.x;
  asm volatile("" :: "s"(ssmk), "s"(ssmD), "s"(ws));
  if (__builtin_expect(b >= D, 0)) {
    if (b >= D + NW1) w2_tasks(b - D - NW1, wd, ws, sm); else w1_strip(b - D, wg, wu, ws, sm);
    return;
  }
  const int d = b, lane = tid & 63, w = __builtin_amdgcn_readfirstlane(tid >> 6);
  const int l15 = lane & 15, q = lane >> 4;
  const rsrc_t rk = mkrsrc(ssmk + d * S, S * 2), rs = mkrsrc(ws, WS_END);
  const u32x4 c0 = ld128nt(rk, 16 * tid), c1 = ld128nt(rk, 16 * tid + 16);
  const u32x4 uu = ld128(rs, WS_URT + d * (S * 2) + 16 * tid);
  u32x2 xv[2]; u32x4 st[2];
#pragma unroll
  for (int i = 0; i < 2; ++i) {
    const int t = 256 * (2 * w + i) + 16 * l15 + 4 * q;
    xv[i] = ld64(rs, WS_XT + (d * S + t) * 2);
    st[i] = ld128(rs, WS_ST1 + t * 4);
  }
  const float Dd = blo(ssmD[d]);
  pin();
  auto build = [&](int m, const u32x4& a, const u32x4& bb) {
    const u32 ww[8] = {a[0], a[1], a[2], a[3], bb[0], bb[1], bb[2], bb[3]};
    u32x4 o0, o1, o2, o3;
#pragma unroll
    for (int i = 0; i < 4; ++i) {
      o0[i] = __builtin_amdgcn_alignbyte(ww[i + 1], ww[i], 2); o1[i] = ww[i + 1];
      o2[i] = __builtin_amdgcn_alignbyte(ww[i + 2], ww[i + 1], 2); o3[i] = ww[i + 2];
    }
    *(u32x4*)(sm + 0 * KC_STRIDE + 16 * m) = o0; *(u32x4*)(sm + 1 * KC_STRIDE + 16 * m) = o1;
    *(u32x4*)(sm + 2 * KC_STRIDE + 16 * m) = o2; *(u32x4*)(sm + 3 * KC_STRIDE + 16 * m) = o3;
  };
  const u32x4 zero4 = {0, 0, 0, 0};
  build(tid + 2, c0, c1);
  if (tid < 2) build(1 - tid, zero4, tid == 0 ? c0 : zero4);
  *(u32x4*)(sm + K1_U + 16 * tid) = uu;
  if (tid < 32) *(u32x4*)(sm + K1_U + 4096 + 16 * tid) = zero4;
  __syncthreads();
  f32x4 acc[8];
#pragma unroll
  for (int j = 0; j < 8; ++j) acc[j] = f32x4{0, 0, 0, 0};
#pragma unroll
  for (int rr = 0; rr < 2; ++rr) {
    const int rho = 2 * w + rr;
    const char* pf = sm + (l15 & 3) * KC_STRIDE + 2 * (l15 & 12) + 16 * q + 64 * rho;
    const char* pg = sm + K1_U + 480 - 32 * l15 + 64 * rho + 16 * q;
    u32x4 Ff[8], Gf[8];
#pragma unroll
    for (int i = 0; i < 8; ++i) {
      Gf[i] = *(const u32x4*)(pg + 512 * (7 - i));
      const u32x2 a = *(const volatile LAS u32x2*)(pf + 512 * i), bb = *(const volatile LAS u32x2*)(pf + 512 * i + 8);
      Ff[i] = u32x4{a[0], a[1], bb[0], bb[1]};
    }
#pragma unroll
    for (int i = 0; i < 8; ++i)
#pragma unroll
      for (int e = 0; e + i < 8; ++e) acc[i + e] = mfma16(Ff[i], Gf[e], acc[i + e]);
  }
  __syncthreads();
  f32x4 own[2];
  {
    char* p = sm + w * 1024 + lane * 16;
#pragma unroll
    for (int j = 0; j < 8; ++j) if ((j >> 1) != w) *(f32x4*)(p + j * 4096) = acc[j];
#pragma unroll
    for (int i = 0; i < 2; ++i) own[i] = w == 0 ? acc[i] : w == 1 ? acc[2 + i] : w == 2 ? acc[4 + i] : acc[6 + i];
  }
  __syncthreads();
#pragma unroll
  for (int i = 0; i < 2; ++i) {
    const int j = 2 * w + i;
    f32x4 y = own[i];
#pragma unroll
    for (int s = 1; s < 4; ++s) y += *(const f32x4*)(sm + (j * 4 + ((w + s) & 3)) * 1024 + lane * 16);
    const int t = 256 * j + 16 * l15 + 4 * q;
    const u32x2 ur = *(const u32x2*)(sm + K1_U + 2 * (2044 - t));
    const float uv[4] = {bhi(ur[1]), blo(ur[1]), bhi(ur[0]), blo(ur[0])};
    const float xx[4] = {blo(xv[i][0]), bhi(xv[i][0]), blo(xv[i][1]), bhi(xv[i][1])};
    float h[4];
#pragma unroll
    for (int r = 0; r < 4; ++r) {
      const float ln = (xx[r] - bhi(st[i][r])) * __builtin_amdgcn_rcpf(blo(st[i][r]));
      h[r] = fmaf(ln, fmaf(uv[r], Dd, y[r]), xx[r]);
    }
    st64<0>(rs, WS_HT + (d * S + t) * 2, u32x2{pk2(h[0], h[1]), pk2(h[2], h[3])});
  }
}

template <int N> DI void waitv() { asm volatile("s_waitcnt vmcnt(%0)" ::"n"(N) : "memory"); }
DI void dma16(u32x4 srd, u32 lds, u32 voff, u32 soff) {
  asm volatile("s_mov_b32 m0, %0\n\ts_nop 0\n\tbuffer_load_dwordx4 %1, %2, %3 offen lds" ::"s"(lds), "v"(voff), "s"(srd), "s"(soff) : "memory");
}
DI u32x4 mksrd(const void* p, u32 bytes) { const unsigned long long a = (unsigned long long)p; return u32x4{(u32)a, (u32)(a >> 32) & 0xffffu, bytes, 0x00020000u}; }
DI u32 ldsaddr(const char* p) { return (u32)(unsigned long long)(LAS const char*)p; }
DI void bar() { __builtin_amdgcn_s_barrier(); }

template <int CNT, int K> DI void wait_big(bool big) { if (big) waitv<CNT * K>(); else waitv<(CNT - 1) * K>(); }
template <int CNT, int K> DI void tail_waits(bool big) { wait_big<CNT, K>(big); bar(); if constexpr (K > 0) tail_waits<CNT, K - 1>(big); }
template <int CNT, int NS, int NST, class OFF>
struct Loader {
  u32x4 srd; u32 lb; u32 voff[CNT], sstep[CNT], ldst[CNT]; bool big;
  static DI u32 slot_off(int j) { return (u32)OFF::at(j); }
  DI void issue(int st, int j) {
    const u32 slot = slot_off(j);
#pragma unroll
    for (int i = 0; i < CNT; ++i) if (i < CNT - 1 || big) dma16(srd, lb + slot + ldst[i], voff[i], st * sstep[i]);
  }
  template <int S0, int S1> DI void prologue() {
#pragma unroll
    for (int s = S0; s < S1; ++s) issue(s, s);
  }
  DI void run() {
    static_assert(CNT * (NS - 3) <= 63, "vmcnt");
    wait_big<CNT, NS - 3>(big);
    bar();
    issue(NS - 2, NS - 2);
    int j = NS - 1;
    for (int n = 0; n + NS - 1 < NST; ++n) {
      wait_big<CNT, NS - 3>(big);
      bar();
      issue(n + NS - 1, j);
      j = j == NS - 1 ? 0 : j + 1;
    }
    tail_waits<CNT, NS - 3>(big);
  }
};

constexpr int G1_NS = 6, G1_SLOT = 22 * 1024, G1_NST = 20;
constexpr int G2_NS = 12, G2_SLOT = 10 * 1024, G2_RING = 2 * G1_SLOT, G2_NST = 27, G2_PRE = 10;
DI constexpr int g2_off(int j) { return j < G2_PRE ? G2_RING + j * G2_SLOT : (j - G2_PRE) * G2_SLOT; }
struct OffA { static DI constexpr int at(int j) { return j * G1_SLOT; } };
struct OffB { static DI constexpr int at(int j) { return g2_off(j); } };
constexpr int G2_HW = 5;
constexpr int G_AUX = G2_RING + G2_PRE * G2_SLOT, G_X0 = G2_RING + 8 * G2_SLOT, G_X1 = G_X0 + 8 * 1024, G_LDS = G_AUX + 10 * 1024;
static_assert(G_AUX >= G1_NS * G1_SLOT && G_LDS <= 160 * 1024, "");
static_assert((G2_NS - G2_PRE) * G2_SLOT <= G2_RING, "phase B ring lies in the slots that phase A has left");
typedef __attribute__((address_space(1))) u32 gu32;

DI void mlp_loader(char* sm, char* ws, int rt, int c, int rt3, int dg, int lw, int lane) {
  constexpr int CNTA = 6, CNTB = 3;
  const u32x4 srd = mksrd(ws, WS_END);
  const u32 lb = ldsaddr(sm);
  {
    Loader<CNTA, G1_NS, G1_NST, OffA> L;
    L.srd = srd; L.lb = lb; L.big = lw < 2;
    const int l15 = lane & 15, q = lane >> 4;
#pragma unroll
    for (int i = 0; i < CNTA; ++i) {
      const int p = lw + 4 * i;
      L.ldst[i] = p * 1024;
      if (p < 14) { L.voff[i] = WS_W1T + (c * 20 * 14 + p) * 1024 + lane * 16; L.sstep[i] = 14 * 1024; }
      else { const int kk = 4 * (p - 14) + q, f = (kk & 3) | (((kk >> 3) & 1) << 2);
             L.voff[i] = WS_HT + kk * (S * 2) + rt * 256 + 16 * (l15 ^ (f << 1)); L.sstep[i] = 32 * S * 2; }
    }
    L.template prologue<0, G1_NS - 2>();
    L.run();
  }
#pragma unroll
  for (int i = 0; i < 3; ++i) {
    const int p = lw + 4 * i;
    if (p < 10) dma16(srd, lb + G_AUX + p * 1024, WS_HT + (80 * dg + 8 * p + (lane >> 3)) * (S * 2) + rt3 * 128 + (lane & 7) * 16, 0);
  }
  Loader<CNTB, G2_NS, G2_NST, OffB> L;
  L.srd = srd; L.lb = lb; L.big = lw < 2;
#pragma unroll
  for (int i = 0; i < CNTB; ++i) { const int p = lw + 4 * i; L.ldst[i] = p * 1024; L.voff[i] = WS_W2T + (dg * 54 * 5 + p) * 1024 + lane * 16; L.sstep[i] = 10 * 1024;
  }
  L.template prologue<0, 4>();
  bar();
  L.template prologue<4, G2_NS - 2>();
  L.run();
  __syncthreads();
}

template <int NT, int T0, bool STAT>
DI void g1_body(char* sm, char* ws, int rt, int c, int w, int lane) {
  const int tw = w & 3, l15 = lane & 15, q = lane >> 4;
  const int qp = l15 >> 2, p4 = lane & 3, msk = 2 * qp | ((q & 1) << 3);
  int hoff[2];
#pragma unroll
  for (int i = 0; i < 2; ++i) hoff[i] = 14336 + (8 * q + qp) * 256 + 16 * ((4 * tw + 2 * i + (p4 >> 1)) ^ msk) + 8 * (p4 & 1);
  const int woff = T0 * 1024 + lane * 16;
  u32x4 W[NT];
  u32x2 h0a, h0b, h1a, h1b;
  f32x4 acc[2][NT], accS[2], accG[2];
#pragma unroll
  for (int i = 0; i < 2; ++i) {
    accS[i] = f32x4{0, 0, 0, 0}; accG[i] = f32x4{0, 0, 0, 0};
#pragma unroll
    for (int m = 0; m < NT; ++m) acc[i][m] = f32x4{0, 0, 0, 0};
  }
  const u32x4 ones = {0x3f803f80u, 0x3f803f80u, 0x3f803f80u, 0x3f803f80u};
  auto half0 = [&](const char* slot) {
    const u32x4 h0 = {h0a[0], h0a[1], h0b[0], h0b[1]};
#pragma unroll
    for (int m = 0; m < NT; ++m) {
      acc[0][m] = mfma16(W[m], h0, acc[0][m]);
      if (m == 0) h1a = trd(slot + hoff[1]);
      if (m == 1) h1b = trd(slot + hoff[1] + 1024);
      pin();
    }
    if (STAT) { accS[0] = mfma16(ones, h0, accS[0]); accG[0] = mfma16(h0, h0, accG[0]); pin(); }
  };
  auto half1 = [&](const char* nslot, bool doread) {
    const u32x4 h1 = {h1a[0], h1a[1], h1b[0], h1b[1]};
#pragma unroll
    for (int m = 0; m < NT; ++m) {
      acc[1][m] = mfma16(W[m], h1, acc[1][m]);
      if (doread) {
        W[m] = *(const u32x4*)(nslot + woff + m * 1024);
        if (m == 0) h0a = trd(nslot + hoff[0]);
        if (m == 1) h0b = trd(nslot + hoff[0] + 1024);
      }
      pin();
    }
    if (STAT) { accS[1] = mfma16(ones, h1, accS[1]); accG[1] = mfma16(h1, h1, accG[1]); pin(); }
  };
  bar();
#pragma unroll
  for (int m = 0; m < NT; ++m) {
    W[m] = *(const u32x4*)(sm + woff + m * 1024);
    if (m == 0) h0a = trd(sm + hoff[0]);
    if (m == 1) h0b = trd(sm + hoff[0] + 1024);
  }
  u32 cur = 0;
  half0(sm);
  for (int n = 0; n < G1_NST - 1; ++n) {
    bar();
    cur = cur == (G1_NS - 1) * G1_SLOT ? 0u : cur + G1_SLOT;
    half1(sm + cur, true);
    half0(sm + cur);
  }
  half1(sm, false);
  const rsrc_t rs = mkrsrc(ws, WS_END);
  const int ksb = 3 * c;
#pragma unroll
  for (int i = 0; i < 2; ++i) {
    const u32 ttile = 8 * rt + 2 * tw + i;
    u32 o[NT / 2][2];
#pragma unroll
    for (int pr = 0; pr < NT / 2; ++pr) {
      const f32x4 g = acc[i][2 * pr], u = acc[i][2 * pr + 1];
      o[pr][0] = pk2(fmaxf(g[0], 0.f) * u[0], fmaxf(g[1], 0.f) * u[1]); o[pr][1] = pk2(fmaxf(g[2], 0.f) * u[2], fmaxf(g[3], 0.f) * u[3]);
    }
    if (NT == 8) {
      st128(rs, WS_HM + ((ttile * 54 + ksb) * 64 + lane) * 16, u32x4{o[0][0], o[0][1], o[1][0], o[1][1]});
      st128(rs, WS_HM + ((ttile * 54 + ksb + 1) * 64 + lane) * 16, u32x4{o[2 % (NT / 2)][0], o[2 % (NT / 2)][1], o[3 % (NT / 2)][0], o[3 % (NT / 2)][1]});
    } else {
      st128(rs, WS_HM + ((ttile * 54 + ksb + 2) * 64 + lane) * 16, u32x4{o[0][0], o[0][1], o[1][0], o[1][1]});
      if (NT == 6) st64(rs, WS_HM + ((ttile * 54 + 48 + (c >> 1)) * 64 + lane) * 16 + 8 * (c & 1), u32x2{o[2 % (NT / 2)][0], o[2 % (NT / 2)][1]});
    }
    if (STAT) {
      if (tw == c - 12 && q == (l15 >> 2)) {
        const int r = l15 & 3;
        const float s2 = r == 0 ? accG[i][0] : r == 1 ? accG[i][1] : r == 2 ? accG[i][2] : accG[i][3];
        const float mean = accS[i][0] * (1.f / D), var = fmaxf(s2 * (1.f / D) - mean * mean, 0.f);
        const float rinv = __builtin_amdgcn_rcpf(rbf(__builtin_amdgcn_sqrtf(rbf(rbf(var) + EPS))));
        __builtin_amdgcn_raw_buffer_store_b32(__float_as_uint(rinv * rinv), rs, WS_R2 + (16 * ttile + l15) * 4, 0, AUX_WT);
      }
    }
  }
}

DI void g2_body(char* sm, char* ws, u16* out, int rt3, int dg, int w, int lane) {
  const int tt = w & 3, kh = w >> 2, l15 = lane & 15, q = lane >> 4;
  const rsrc_t rs = mkrsrc(ws, WS_END);
  const u32 hbase = WS_HM + ((4 * rt3 + tt) * 54 + kh) * 1024 + lane * 16;
  u32x4 H[G2_HW];
  auto hload = [&](int n) { return __builtin_amdgcn_raw_buffer_load_b128(rs, hbase, n * 2048, AUX_WT); };
#pragma unroll
  for (int j = 0; j < G2_HW; ++j) H[j] = hload(j);
  const float r2 = __uint_as_float(__builtin_amdgcn_raw_buffer_load_b32(rs, WS_R2 + (64 * rt3 + 16 * tt + l15) * 4, 0, AUX_WT));
  pin();
  const char* wb = sm + kh * 5 * 1024 + lane * 16;
  u32x4 Wa[5], Wb[5];
  f32x4 acc[5];
#pragma unroll
  for (int m = 0; m < 5; ++m) { acc[m] = f32x4{0, 0, 0, 0}; Wa[m] = *(const u32x4*)(wb + g2_off(0) + m * 1024); }
  auto step = [&](u32x4 (&Wc)[5], u32x4 (&Wn)[5], int n) {
    if (n + 1 < G2_NST) bar();
#pragma unroll
    for (int m = 0; m < 5; ++m) {
      if (n + 1 < G2_NST) Wn[m] = *(const u32x4*)(wb + g2_off((n + 1) % G2_NS) + m * 1024);
      acc[m] = mfma16(Wc[m], H[n % G2_HW], acc[m]);
      pin();
    }
    if (n + G2_HW < G2_NST) { H[n % G2_HW] = hload(n + G2_HW); pin(); }
  };
#pragma unroll
  for (int n = 0; n < G2_NST; ++n) { if (n & 1) step(Wb, Wa, n); else step(Wa, Wb, n);
  }
  char* e0 = sm + G_X0 + tt * 2048 + lane * 16;
  char* e1 = sm + G_X1 + tt * 3072 + lane * 16;
  if (kh == 0) { *(f32x4*)e0 = acc[3]; *(f32x4*)(e0 + 1024) = acc[4]; }
  else { *(f32x4*)e1 = acc[0]; *(f32x4*)(e1 + 1024) = acc[1]; *(f32x4*)(e1 + 2048) = acc[2]; }
  __syncthreads();
  const char* res = sm + G_AUX + (4 * q + (l15 >> 2)) * 128 + (16 * tt + 4 * (lane & 3)) * 2;
  auto fin = [&](int m, const f32x4& o) {
    const u32x2 h = trd(res + m * 16 * 128);
    const float r0 = blo(h[0]) + rbf(o[0] * r2), r1 = bhi(h[0]) + rbf(o[1] * r2), r2b = blo(h[1]) + rbf(o[2] * r2), r3 = bhi(h[1]) + rbf(o[3] * r2);
    *(u32x2*)(out + (64 * rt3 + 16 * tt + l15) * D + 80 * dg + 16 * m + 4 * q) = u32x2{pk2(r0, r1), pk2(r2b, r3)};
  };
  if (kh == 0) {
#pragma unroll
    for (int m = 0; m < 3; ++m) fin(m, acc[m] + *(const f32x4*)(e1 + m * 1024));
  } else {
#pragma unroll
    for (int m = 3; m < 5; ++m) fin(m, acc[m] + *(const f32x4*)(e0 + (m - 3) * 1024));
  }
}

constexpr int MLP_THREADS = 768;
extern "C" __global__ __launch_bounds__(MLP_THREADS, 3) void k2_mlp(char* __restrict__ ws, u16* __restrict__ out) {
  extern __shared__ __attribute__((aligned(16))) char sm[];
  const int b = blockIdx.x, x = b & 7, i = b >> 3, cq = x >> 1;
  const int rt = 8 * (x & 1) + (i & 7), c = 4 * cq + (i >> 3);
  const int rt3 = 2 * rt + (cq >> 1), dg = 4 * (cq & 1) + (c & 3);
  const int lane = threadIdx.x & 63, w = __builtin_amdgcn_readfirstlane(threadIdx.x >> 6);
  if (__builtin_expect(w >= 8, 1)) { mlp_loader(sm, ws, rt, c, rt3, dg, w - 8, lane); return; }
  if (w < 4) g1_body<8, 0, false>(sm, ws, rt, c, w, lane);
  else if (c < 12) g1_body<6, 8, false>(sm, ws, rt, c, w, lane);
  else g1_body<4, 8, true>(sm, ws, rt, c, w, lane);
  asm volatile("s_waitcnt vmcnt(0)" ::: "memory");
  bar();
  if (threadIdx.x == 0) {
    gu32* cnt = (gu32*)(ws + WS_CNT) + (rt3 >> 1) * 32;
    __hip_atomic_fetch_add((gu32*)(ws + WS_CNT) + rt * 32, 1u, __ATOMIC_RELAXED, __HIP_MEMORY_SCOPE_AGENT);
    for (u32 spins = 0; __hip_atomic_load(cnt, __ATOMIC_RELAXED, __HIP_MEMORY_SCOPE_AGENT) < 16u && spins < (1u << 20); ++spins) __builtin_amdgcn_s_sleep(1);
  }
  __builtin_amdgcn_fence(__ATOMIC_ACQUIRE, "wavefront");
  bar();
  g2_body(sm, ws, out, rt3, dg, w, lane);
}

extern "C" __global__ void k9_end() {}

extern "C" void kernel_launch(void* const* d_in, const int* in_sizes, int n_in, void* d_out, int out_size, void* d_ws, size_t ws_size, hipStream_t stream) {
  const u16* q = (const u16*)d_in[0]; const u16* k = (const u16*)d_in[1]; const u16* v = (const u16*)d_in[2];
  const u16* shk = (const u16*)d_in[3]; const u16* ssmk = (const u16*)d_in[4]; const u16* ssmD = (const u16*)d_in[5];
  const u16* wg = (const u16*)d_in[7]; const u16* wu = (const u16*)d_in[8]; const u16* wd = (const u16*)d_in[9];
  char* ws = (char*)d_ws;
  (void)hipFuncSetAttribute((const void*)k2_mlp, hipFuncAttributeMaxDynamicSharedMemorySize, G_LDS);
  k0_prep<<<456, 256, 0, stream>>>(q, k, v, shk, ws);
  k1_conv<<<D + NW1 + NW2, 256, K1_LDS, stream>>>(ssmk, ssmD, wg, wu, wd, ws);
  k2_mlp<<<256, MLP_THREADS, G_LDS, stream>>>(ws, (u16*)d_out);
}
```
